```python
import torch

def _softmax(x, axis=-1):
    return torch.softmax(x, dim=axis)
def _gelu(x):
    return torch.nn.functional.gelu(x, approximate="tanh")
def _silu(x):
    return torch.nn.functional.silu(x)
def _sigmoid(x):
    return torch.sigmoid(x)
def _layernorm(x, axis=-1, eps=1e-5):
    m = x.mean(dim=axis, keepdim=True)
    v = x.var(dim=axis, keepdim=True, unbiased=False)
    return (x - m) / torch.sqrt(v + eps)
def _scatter_add(tgt, idx, src, axis=0):
    return tgt.clone().index_add_(axis, idx, src)
def _segment_sum(src, seg, n):
    out = torch.zeros((n,) + src.shape[1:], dtype=src.dtype)
    return out.index_add_(0, seg, src)
def _pad(x, pw):
    flat = tuple(v for pair in reversed(pw) for v in pair)
    return torch.nn.functional.pad(x, flat)

kernel_name = 'parallel_sum__linear_kernel_hedgehog__glu_gated_reglu__prompt'

def setup_inputs():

    _w = (lambda a: (lambda t: t.to(torch.bfloat16) if t.is_floating_point() else t)(torch.as_tensor(a)))
    def _A_setup():
        B, S, D, H = 1, 2048, 512, 32
        Dh = D // H
        return {
            "x": _w((lambda *s: torch.randn(*s, dtype=torch.bfloat16) * 0.05)(B, S, D)),
            "wqkv": _w((lambda *s: torch.randn(*s, dtype=torch.bfloat16) * 0.2)(D, 3 * D)),
            "w_phi": _w((lambda *s: torch.randn(*s, dtype=torch.bfloat16) * 0.2)(H, Dh, Dh) * (Dh ** -0.5)),
            "wo": _w((lambda *s: torch.randn(*s, dtype=torch.bfloat16) * 0.05)(D, D)),
        }
    _A_in = _A_setup()

    def _B_setup():
        B, S, D, F = 1, 2048, 512, 1792
        return {
            "x": _w((lambda *s: torch.randn(*s, dtype=torch.bfloat16) * 0.05)(B, S, D)),
            "W_gate": _w((lambda *s: torch.randn(*s, dtype=torch.bfloat16) * 0.4)(D, F) * (2.0 / D) ** 0.5),
            "W_up": _w((lambda *s: torch.randn(*s, dtype=torch.bfloat16) * 0.4)(D, F) * (2.0 / D) ** 0.5),
            "W_down": _w((lambda *s: torch.randn(*s, dtype=torch.bfloat16) * 0.4)(F, D) * (1.0 / F) ** 0.5),
        }
    _B_in = _B_setup()

    return {
        "A_x": _A_in["x"],
        "A_wqkv": _A_in["wqkv"],
        "A_w_phi": _A_in["w_phi"],
        "A_wo": _A_in["wo"],
        "B_x": _B_in["x"],
        "B_W_gate": _B_in["W_gate"],
        "B_W_up": _B_in["W_up"],
        "B_W_down": _B_in["W_down"],
    }

def reference(A_x, A_wqkv, A_w_phi, A_wo, B_x, B_W_gate, B_W_up, B_W_down):
    def _A_ref(x, wqkv, w_phi, wo):
        B, S, D = x.shape
        H = 32; Dh = D // H
        qkv = (lambda x, *d: x.permute(*d))((x @ wqkv).reshape(B, S, 3, H, Dh), 2, 0, 3, 1, 4)
        q, k, v = qkv[0], qkv[1], qkv[2]
        pq = torch.einsum("bhsd,hdf->bhsf", q, w_phi)
        pk = torch.einsum("bhsd,hdf->bhsf", k, w_phi)
        phi_q = torch.concatenate([_softmax(pq, axis=-1), _softmax(-pq, axis=-1)], axis=-1)
        phi_k = torch.concatenate([_softmax(pk, axis=-1), _softmax(-pk, axis=-1)], axis=-1)
        kv = torch.einsum("bhsf,bhsd->bhfd", phi_k, v)
        z  = torch.einsum("bhsf->bhf", phi_k)
        num = torch.einsum("bhqf,bhfd->bhqd", phi_q, kv)
        den = torch.einsum("bhqf,bhf->bhq", phi_q, z)[..., None] + 1e-6
        o = num / den
        return (lambda x, *d: x.permute(*d))(o, 0, 2, 1, 3).reshape(B, S, D) @ wo

    def _B_ref(x, W_gate, W_up, W_down):
        gate = torch.einsum("bsd,df->bsf", x, W_gate)
        up = torch.einsum("bsd,df->bsf", x, W_up)
        gate = gate * (lambda x, dt: x.to(dt))(gate > 0, gate.dtype)
        h = gate * up
        return torch.einsum("bsf,fd->bsd", h, W_down)

    x = A_x
    xn = _layernorm(x, axis=-1)
    return x + _A_ref(xn, A_wqkv, A_w_phi, A_wo) + _B_ref(xn, B_W_gate, B_W_up, B_W_down)
```

```cpp
#include <hip/hip_runtime.h>
#include <hip/hip_bf16.h>
#include <stdint.h>

typedef short bf16x8 __attribute__((ext_vector_type(8)));
typedef short bf16x4 __attribute__((ext_vector_type(4)));
typedef float f32x4 __attribute__((ext_vector_type(4)));
typedef unsigned short u16;
typedef __attribute__((address_space(3))) bf16x4* lds_b4_ptr;

constexpr int S = 2048, D = 512, H = 32, F = 1792;
constexpr int NQKV = 3 * D;

constexpr size_t WS_XN = 0;
constexpr size_t WS_PHIQ = WS_XN + (size_t)S * D * 2;
constexpr size_t WS_H = WS_PHIQ + (size_t)S * 1024 * 2;
constexpr size_t WS_KV = WS_H + (size_t)S * F * 2;
constexpr int KV_FLOATS = H * 32 * 16 + H * 32;

__device__ __forceinline__ float bf2f(u16 v) { return __uint_as_float(((unsigned)v) << 16); }
__device__ __forceinline__ u16 f2bf(float f) {
  __hip_bfloat16 h = __float2bfloat16(f);
  return *reinterpret_cast<u16*>(&h);
}
__device__ __forceinline__ bf16x4 pack4(float a, float b, float c, float d) {
  bf16x4 r;
  r[0] = (short)f2bf(a); r[1] = (short)f2bf(b); r[2] = (short)f2bf(c); r[3] = (short)f2bf(d);
  return r;
}
__device__ __forceinline__ void glds16(const void* g, void* l) {
  __builtin_amdgcn_global_load_lds((const unsigned*)g, (unsigned*)l, 16, 0, 0);
}
__device__ __forceinline__ bf16x4 tr_read(const char* p) {
  return __builtin_amdgcn_ds_read_tr16_b64_v4i16((lds_b4_ptr)p);
}
__device__ __forceinline__ bf16x8 cat8(bf16x4 a, bf16x4 b) {
  return (bf16x8){a[0], a[1], a[2], a[3], b[0], b[1], b[2], b[3]};
}
template <int CTRL> __device__ __forceinline__ float dppf(float v) {
  return __int_as_float(__builtin_amdgcn_update_dpp(0, __float_as_int(v), CTRL, 0xF, 0xF, true));
}
__device__ __forceinline__ float red16_max(float v) {
  v = fmaxf(v, dppf<0xB1>(v)); v = fmaxf(v, dppf<0x4E>(v));
  v = fmaxf(v, dppf<0x124>(v)); v = fmaxf(v, dppf<0x128>(v)); return v;
}
__device__ __forceinline__ float red16_sum(float v) {
  v += dppf<0xB1>(v); v += dppf<0x4E>(v);
  v += dppf<0x124>(v); v += dppf<0x128>(v); return v;
}

__global__ __launch_bounds__(256) void prep_kernel(const u16* __restrict__ x, u16* __restrict__ xn, float* __restrict__ kvacc) {
  const int wave = (blockIdx.x * 256 + threadIdx.x) >> 6, lane = threadIdx.x & 63;
  const int gid = blockIdx.x * 256 + threadIdx.x;
  if (gid < KV_FLOATS / 4) reinterpret_cast<f32x4*>(kvacc)[gid] = (f32x4){0.f, 0.f, 0.f, 0.f};
  bf16x8 v = *reinterpret_cast<const bf16x8*>(x + (size_t)wave * D + lane * 8);
  float f[8]; float s = 0.f;
#pragma unroll
  for (int j = 0; j < 8; ++j) { f[j] = bf2f((u16)v[j]); s += f[j]; }
#pragma unroll
  for (int o = 32; o > 0; o >>= 1) s += __shfl_xor(s, o);
  const float mean = s * (1.f / D);
  float q = 0.f;
#pragma unroll
  for (int j = 0; j < 8; ++j) { f[j] -= mean; q += f[j] * f[j]; }
#pragma unroll
  for (int o = 32; o > 0; o >>= 1) q += __shfl_xor(q, o);
  const float rstd = rsqrtf(q * (1.f / D) + 1e-5f);
  bf16x8 r;
#pragma unroll
  for (int j = 0; j < 8; ++j) r[j] = (short)f2bf(f[j] * rstd);
  *reinterpret_cast<bf16x8*>(xn + (size_t)wave * D + lane * 8) = r;
}

constexpr int G1_ABYTES = 128 * 128;
constexpr int G1_BBYTES = 64 * 256;
constexpr int G1_STAGE = G1_ABYTES + G1_BBYTES;

template <int TYPE>
__device__ __forceinline__ void gemm1_body(char* smem, int m0, int nt,
                                           const u16* __restrict__ xn, const u16* __restrict__ wqkv,
                                           const u16* __restrict__ wphi, const u16* __restrict__ wg,
                                           const u16* __restrict__ wu, u16* __restrict__ phiq,
                                           u16* __restrict__ hbuf, float* __restrict__ kvacc) {
  const int tid = threadIdx.x, w = tid >> 6, l = tid & 63, wm = w >> 1, wn = w & 1;
  const int g = l >> 4, r16 = l & 15;

  const u16* asrc[4]; const u16* bsrc[4];
  constexpr int LDB = (TYPE == 2) ? F : NQKV;
#pragma unroll
  for (int i = 0; i < 4; ++i) {
    const int row = w * 32 + i * 8 + (l >> 3);
    const int ch = (l & 7) ^ ((row >> 1) & 7);
    asrc[i] = xn + (size_t)(m0 + row) * D + ch * 8;
    const int krow = w * 16 + i * 4 + g;
    const int bch = r16 ^ (((krow & 3) << 2) | ((krow >> 2) & 3));
    const u16* base;
    if (TYPE == 0) base = wqkv + nt * 128 + bch * 8;
    else if (TYPE == 1) {
      const int ww = bch >> 3, ni = (bch >> 1) & 3, off = (bch & 1) * 8;
      const int h = (nt - 4) * 4 + ww * 2 + (ni & 1);
      base = wqkv + ((ni < 2) ? D : 2 * D) + h * 16 + off;
    } else {
      const int ww = bch >> 3, ni = (bch >> 1) & 3, off = (bch & 1) * 8;
      const int f0 = (nt - 12) * 64 + ww * 32 + (ni & 1) * 16 + off;
      base = ((ni < 2) ? wg : wu) + f0;
    }
    bsrc[i] = base + (size_t)krow * LDB;
  }
  auto stage = [&](int buf, int kt) {
    char* sa = smem + buf * G1_STAGE; char* sb = sa + G1_ABYTES;
#pragma unroll
    for (int i = 0; i < 4; ++i) glds16(asrc[i] + kt * 64, sa + (w * 32 + i * 8) * 128);
#pragma unroll
    for (int i = 0; i < 4; ++i) glds16(bsrc[i] + (size_t)kt * 64 * LDB, sb + (w * 16 + i * 4) * 256);
  };

  f32x4 acc[4][4];
#pragma unroll
  for (int a = 0; a < 4; ++a)
#pragma unroll
    for (int b = 0; b < 4; ++b) acc[a][b] = (f32x4){0.f, 0.f, 0.f, 0.f};

  const int q4 = r16 >> 2, p4 = l & 3;
  auto compute = [&](int buf) {
    const char* sa = smem + buf * G1_STAGE; const char* sb = sa + G1_ABYTES;
#pragma unroll
    for (int kk = 0; kk < 2; ++kk) {
      bf16x8 xa[4], wb[4];
#pragma unroll
      for (int mi = 0; mi < 4; ++mi) {
        const int row = wm * 64 + mi * 16 + r16;
        xa[mi] = *reinterpret_cast<const bf16x8*>(sa + row * 128 + (((kk * 4 + g) ^ ((row >> 1) & 7)) << 4));
      }
#pragma unroll
      for (int ni = 0; ni < 4; ++ni) {
        const int c = wn * 4 + ni;
        bf16x4 t[2];
#pragma unroll
        for (int tt = 0; tt < 2; ++tt) {
          const int row = 32 * kk + 8 * g + 4 * tt + q4;
          const int ch = 2 * c + (p4 >> 1);
          t[tt] = tr_read(sb + 256 * row + ((ch ^ (((row & 3) << 2) | ((row >> 2) & 3))) << 4) + 8 * (p4 & 1));
        }
        wb[ni] = cat8(t[0], t[1]);
      }
#pragma unroll
      for (int mi = 0; mi < 4; ++mi)
#pragma unroll
        for (int ni = 0; ni < 4; ++ni) {
          if (TYPE == 1 && ni >= 2) acc[mi][ni] = __builtin_amdgcn_mfma_f32_16x16x32_bf16(xa[mi], wb[ni], acc[mi][ni], 0, 0, 0);
          else acc[mi][ni] = __builtin_amdgcn_mfma_f32_16x16x32_bf16(wb[ni], xa[mi], acc[mi][ni], 0, 0, 0);
        }
    }
  };

  stage(0, 0);
  __syncthreads();
#pragma unroll 1
  for (int kt = 0; kt < 7; ++kt) {
    stage((kt + 1) & 1, kt + 1);
    compute(kt & 1);
    __syncthreads();
  }
  compute(1);

  if (TYPE == 0) {
#pragma unroll
    for (int ni = 0; ni < 4; ++ni) {
      const int h = nt * 8 + wn * 4 + ni;
      bf16x4 wf;
#pragma unroll
      for (int j = 0; j < 4; ++j) wf[j] = (short)wphi[h * 256 + (4 * g + j) * 16 + r16];
#pragma unroll
      for (int mi = 0; mi < 4; ++mi) {
        const bf16x4 qf = pack4(acc[mi][ni][0], acc[mi][ni][1], acc[mi][ni][2], acc[mi][ni][3]);
        f32x4 p = __builtin_amdgcn_mfma_f32_16x16x16bf16_1k(wf, qf, (f32x4){0.f, 0.f, 0.f, 0.f}, 0, 0, 0);
        float mx = fmaxf(fmaxf(p[0], p[1]), fmaxf(p[2], p[3]));
        float mn = fminf(fminf(p[0], p[1]), fminf(p[2], p[3]));
        mx = fmaxf(mx, __shfl_xor(mx, 16)); mx = fmaxf(mx, __shfl_xor(mx, 32));
        mn = fminf(mn, __shfl_xor(mn, 16)); mn = fminf(mn, __shfl_xor(mn, 32));
        float e1[4], e2[4], s1 = 0.f, s2 = 0.f;
#pragma unroll
        for (int j = 0; j < 4; ++j) { e1[j] = __expf(p[j] - mx); e2[j] = __expf(mn - p[j]); s1 += e1[j]; s2 += e2[j]; }
        s1 += __shfl_xor(s1, 16); s1 += __shfl_xor(s1, 32);
        s2 += __shfl_xor(s2, 16); s2 += __shfl_xor(s2, 32);
        const float r1 = 1.f / s1, r2 = 1.f / s2;
        const int tok = m0 + wm * 64 + mi * 16 + r16;
        u16* dst = phiq + (size_t)tok * 1024 + h * 32 + 4 * g;
        *reinterpret_cast<bf16x4*>(dst) = pack4(e1[0] * r1, e1[1] * r1, e1[2] * r1, e1[3] * r1);
        *reinterpret_cast<bf16x4*>(dst + 16) = pack4(e2[0] * r2, e2[1] * r2, e2[2] * r2, e2[3] * r2);
      }
    }
  } else if (TYPE == 1) {
#pragma unroll
    for (int hh = 0; hh < 2; ++hh) {
      const int h = (nt - 4) * 4 + wn * 2 + hh;
      bf16x4 wf;
#pragma unroll
      for (int j = 0; j < 4; ++j) wf[j] = (short)wphi[h * 256 + (4 * g + j) * 16 + r16];
      f32x4 kvp = {0.f, 0.f, 0.f, 0.f}, kvn = {0.f, 0.f, 0.f, 0.f};
      float zp = 0.f, zn = 0.f;
#pragma unroll
      for (int mi = 0; mi < 4; ++mi) {
        const bf16x4 kf = pack4(acc[mi][hh][0], acc[mi][hh][1], acc[mi][hh][2], acc[mi][hh][3]);
        f32x4 p = __builtin_amdgcn_mfma_f32_16x16x16bf16_1k(kf, wf, (f32x4){0.f, 0.f, 0.f, 0.f}, 0, 0, 0);
        float e1[4], e2[4];
#pragma unroll
        for (int j = 0; j < 4; ++j) {
          const float mx = red16_max(p[j]);
          const float mn = -red16_max(-p[j]);
          const float a = __expf(p[j] - mx), b = __expf(mn - p[j]);
          const float s1 = red16_sum(a), s2 = red16_sum(b);
          e1[j] = bf2f(f2bf(a / s1)); e2[j] = bf2f(f2bf(b / s2));
          zp += e1[j]; zn += e2[j];
        }
        const bf16x4 vf = pack4(acc[mi][2 + hh][0], acc[mi][2 + hh][1], acc[mi][2 + hh][2], acc[mi][2 + hh][3]);
        kvp = __builtin_amdgcn_mfma_f32_16x16x16bf16_1k(pack4(e1[0], e1[1], e1[2], e1[3]), vf, kvp, 0, 0, 0);
        kvn = __builtin_amdgcn_mfma_f32_16x16x16bf16_1k(pack4(e2[0], e2[1], e2[2], e2[3]), vf, kvn, 0, 0, 0);
      }
      float* kvh = kvacc + h * 512;
#pragma unroll
      for (int j = 0; j < 4; ++j) {
        atomicAdd(kvh + (4 * g + j) * 16 + r16, kvp[j]);
        atomicAdd(kvh + (16 + 4 * g + j) * 16 + r16, kvn[j]);
      }
      zp += __shfl_xor(zp, 16); zp += __shfl_xor(zp, 32);
      zn += __shfl_xor(zn, 16); zn += __shfl_xor(zn, 32);
      if (l < 16) {
        atomicAdd(kvacc + H * 512 + h * 32 + l, zp);
        atomicAdd(kvacc + H * 512 + h * 32 + 16 + l, zn);
      }
    }
  } else {
#pragma unroll
    for (int ni = 0; ni < 2; ++ni) {
      const int f0 = (nt - 12) * 64 + wn * 32 + ni * 16 + 4 * g;
#pragma unroll
      for (int mi = 0; mi < 4; ++mi) {
        const int tok = m0 + wm * 64 + mi * 16 + r16;
        const f32x4 ga = acc[mi][ni], up = acc[mi][2 + ni];
        *reinterpret_cast<bf16x4*>(hbuf + (size_t)tok * F + f0) =
            pack4(fmaxf(ga[0], 0.f) * up[0], fmaxf(ga[1], 0.f) * up[1], fmaxf(ga[2], 0.f) * up[2], fmaxf(ga[3], 0.f) * up[3]);
      }
    }
  }
}

__global__ __launch_bounds__(256, 2) void gemm1_kernel(const u16* __restrict__ xn, const u16* __restrict__ wqkv,
                                                       const u16* __restrict__ wphi, const u16* __restrict__ wg,
                                                       const u16* __restrict__ wu, u16* __restrict__ phiq,
                                                       u16* __restrict__ hbuf, float* __restrict__ kvacc) {
  __shared__ __attribute__((aligned(16))) char smem[2 * G1_STAGE];
  const int bid = blockIdx.x;
  const int mt = bid & 15, nt = bid >> 4;
  const int m0 = mt * 128;
  if (nt < 4) gemm1_body<0>(smem, m0, nt, xn, wqkv, wphi, wg, wu, phiq, hbuf, kvacc);
  else if (nt < 12) gemm1_body<1>(smem, m0, nt, xn, wqkv, wphi, wg, wu, phiq, hbuf, kvacc);
  else gemm1_body<2>(smem, m0, nt, xn, wqkv, wphi, wg, wu, phiq, hbuf, kvacc);
}

constexpr int G2_OBYTES = 4 * 64 * 256;
constexpr int G2_ABYTES = 64 * 256;
constexpr int G2_BBYTES = 128 * 128;
constexpr int G2_STAGE = G2_ABYTES + G2_BBYTES;
constexpr int G2_SMEM = G2_OBYTES + 2 * G2_STAGE;

__global__ __launch_bounds__(256, 1) void gemm2_kernel(const u16* __restrict__ x, const u16* __restrict__ phiq,
                                                       const u16* __restrict__ hbuf, const float* __restrict__ kvacc,
                                                       const u16* __restrict__ wo, const u16* __restrict__ wd,
                                                       u16* __restrict__ out) {
  extern __shared__ __attribute__((aligned(16))) char smem[];
  char* oimg = smem;
  char* stg = smem + G2_OBYTES;
  const int tid = threadIdx.x, w = tid >> 6, l = tid & 63, g = l >> 4, r16 = l & 15;
  const int bid = blockIdx.x;
  const int mt = bid & 31, nt = bid >> 5;
  const int m0 = mt * 64, n0 = nt * 64;

  int brow[4], bcol[4], arow[4], acol[4];
#pragma unroll
  for (int i = 0; i < 4; ++i) {
    brow[i] = w * 32 + i * 8 + (l >> 3);
    const int s = ((brow[i] >> 1) & 1) | ((brow[i] >> 2) & 2);
    bcol[i] = n0 + (((l & 7) ^ (s << 1)) << 3);
    arow[i] = w * 16 + i * 4 + g;
    acol[i] = (r16 ^ (arow[i] & 15)) << 3;
  }
  auto stage = [&](int buf, int st) {
    char* sa = stg + buf * G2_STAGE; char* sb = sa + G2_ABYTES;
    const u16* wsrc = (st < 4) ? (wo + (size_t)st * 128 * D) : (wd + (size_t)(st - 4) * 128 * D);
#pragma unroll
    for (int i = 0; i < 4; ++i) glds16(wsrc + (size_t)brow[i] * D + bcol[i], sb + (w * 32 + i * 8) * 128);
    if (st >= 4) {
#pragma unroll
      for (int i = 0; i < 4; ++i)
        glds16(hbuf + (size_t)(m0 + arow[i]) * F + (st - 4) * 128 + acol[i], sa + (w * 16 + i * 4) * 256);
    }
  };
  stage(0, 0);

#pragma unroll 1
  for (int hh = 0; hh < 8; ++hh) {
    const int h = w * 8 + hh;
    bf16x8 kvf, zf;
#pragma unroll
    for (int j = 0; j < 8; ++j) {
      kvf[j] = (short)f2bf(kvacc[h * 512 + (8 * g + j) * 16 + r16]);
      zf[j] = (short)f2bf(kvacc[H * 512 + h * 32 + 8 * g + j]);
    }
#pragma unroll
    for (int mi = 0; mi < 4; ++mi) {
      const int row = mi * 16 + r16;
      const bf16x8 pf = *reinterpret_cast<const bf16x8*>(phiq + (size_t)(m0 + row) * 1024 + h * 32 + 8 * g);
      f32x4 num = __builtin_amdgcn_mfma_f32_16x16x32_bf16(kvf, pf, (f32x4){0.f, 0.f, 0.f, 0.f}, 0, 0, 0);
      f32x4 den = __builtin_amdgcn_mfma_f32_16x16x32_bf16(zf, pf, (f32x4){0.f, 0.f, 0.f, 0.f}, 0, 0, 0);
      const float rd = 1.f / (den[0] + 1e-6f);
      const int st = h >> 3, ch = (h & 7) * 2 + (g >> 1);
      *reinterpret_cast<bf16x4*>(oimg + st * (64 * 256) + row * 256 + ((ch ^ (row & 15)) << 4) + (g & 1) * 8) =
          pack4(num[0] * rd, num[1] * rd, num[2] * rd, num[3] * rd);
    }
  }

  f32x4 acc[4][4];
#pragma unroll
  for (int a = 0; a < 4; ++a)
#pragma unroll
    for (int b = 0; b < 4; ++b) acc[a][b] = (f32x4){0.f, 0.f, 0.f, 0.f};
  const int q4 = r16 >> 2, p4 = l & 3;
  auto compute = [&](int buf, int st) {
    const char* sa = (st < 4) ? (oimg + st * (64 * 256)) : (stg + buf * G2_STAGE);
    const char* sb = stg + buf * G2_STAGE + G2_ABYTES;
    bf16x8 xa[4], wb[4];
#pragma unroll
    for (int mi = 0; mi < 4; ++mi) {
      const int row = mi * 16 + r16;
      xa[mi] = *reinterpret_cast<const bf16x8*>(sa + row * 256 + (((w * 4 + g) ^ (row & 15)) << 4));
    }
#pragma unroll
    for (int ni = 0; ni < 4; ++ni) {
      bf16x4 t[2];
#pragma unroll
      for (int tt = 0; tt < 2; ++tt) {
        const int row = 32 * w + 8 * g + 4 * tt + q4;
        const int s = ((row >> 1) & 1) | ((row >> 2) & 2);
        const int ch = 2 * ni + (p4 >> 1);
        t[tt] = tr_read(sb + 128 * row + ((ch ^ (s << 1)) << 4) + 8 * (p4 & 1));
      }
      wb[ni] = cat8(t[0], t[1]);
    }
#pragma unroll
    for (int mi = 0; mi < 4; ++mi)
#pragma unroll
      for (int ni = 0; ni < 4; ++ni)
        acc[mi][ni] = __builtin_amdgcn_mfma_f32_16x16x32_bf16(wb[ni], xa[mi], acc[mi][ni], 0, 0, 0);
  };

  __syncthreads();
#pragma unroll 1
  for (int st = 0; st < 17; ++st) {
    stage((st + 1) & 1, st + 1);
    compute(st & 1, st);
    __syncthreads();
  }
  compute(1, 17);
  __syncthreads();

  float* red = reinterpret_cast<float*>(stg);
#pragma unroll
  for (int mi = 0; mi < 4; ++mi)
#pragma unroll
    for (int ni = 0; ni < 4; ++ni)
      *reinterpret_cast<f32x4*>(red + w * 4096 + (mi * 16 + r16) * 64 + ni * 16 + 4 * g) = acc[mi][ni];
  __syncthreads();
  const int tok = tid >> 2, nc = (tid & 3) * 16;
  float o[16];
#pragma unroll
  for (int j = 0; j < 4; ++j) {
    f32x4 a = *reinterpret_cast<const f32x4*>(red + tok * 64 + nc + j * 4);
#pragma unroll
    for (int ww = 1; ww < 4; ++ww) {
      f32x4 b = *reinterpret_cast<const f32x4*>(red + ww * 4096 + tok * 64 + nc + j * 4);
      a += b;
    }
    o[j * 4 + 0] = a[0]; o[j * 4 + 1] = a[1]; o[j * 4 + 2] = a[2]; o[j * 4 + 3] = a[3];
  }
  const size_t go = (size_t)(m0 + tok) * D + n0 + nc;
#pragma unroll
  for (int hlf = 0; hlf < 2; ++hlf) {
    const bf16x8 xv = *reinterpret_cast<const bf16x8*>(x + go + hlf * 8);
    bf16x8 r;
#pragma unroll
    for (int j = 0; j < 8; ++j) r[j] = (short)f2bf(bf2f((u16)xv[j]) + o[hlf * 8 + j]);
    *reinterpret_cast<bf16x8*>(out + go + hlf * 8) = r;
  }
}

extern "C" void kernel_launch(void* const* d_in, const int* in_sizes, int n_in,
                              void* d_out, int out_size, void* d_ws, size_t ws_size,
                              hipStream_t stream) {
  const u16* x = (const u16*)d_in[0];
  const u16* wqkv = (const u16*)d_in[1];
  const u16* wphi = (const u16*)d_in[2];
  const u16* wo = (const u16*)d_in[3];
  const u16* wg = (const u16*)d_in[5];
  const u16* wu = (const u16*)d_in[6];
  const u16* wd = (const u16*)d_in[7];
  char* ws = (char*)d_ws;
  u16* xn = (u16*)(ws + WS_XN);
  u16* phiq = (u16*)(ws + WS_PHIQ);
  u16* hbuf = (u16*)(ws + WS_H);
  float* kvacc = (float*)(ws + WS_KV);

  prep_kernel<<<S / 4, 256, 0, stream>>>(x, xn, kvacc);
  gemm1_kernel<<<16 * 40, 256, 0, stream>>>(xn, wqkv, wphi, wg, wu, phiq, hbuf, kvacc);
  static bool attr_set = false;
  if (!attr_set) {
    hipFuncSetAttribute((const void*)gemm2_kernel, hipFuncAttributeMaxDynamicSharedMemorySize, G2_SMEM);
    attr_set = true;
  }
  gemm2_kernel<<<256, 256, G2_SMEM, stream>>>(x, phiq, hbuf, kvacc, wo, wd, (u16*)d_out);
}
```

```cpp
#include <hip/hip_runtime.h>
#include <hip/hip_bf16.h>
#include <stdint.h>

typedef short bf16x8 __attribute__((ext_vector_type(8)));
typedef short bf16x4 __attribute__((ext_vector_type(4)));
typedef float f32x4 __attribute__((ext_vector_type(4)));
typedef unsigned short u16;
typedef __attribute__((address_space(3))) bf16x4* lds_b4_ptr;

constexpr int S = 2048, D = 512, H = 32, F = 1792;
constexpr int NQKV = 3 * D;

constexpr size_t WS_XN = 0;
constexpr size_t WS_PHIQ = WS_XN + (size_t)S * D * 2;
constexpr size_t WS_H = WS_PHIQ + (size_t)S * 1024 * 2;
constexpr size_t WS_KV = WS_H + (size_t)S * F * 2;
constexpr int KV_FLOATS = H * 32 * 16 + H * 32;

__device__ __forceinline__ float bf2f(u16 v) { return __uint_as_float(((unsigned)v) << 16); }
__device__ __forceinline__ u16 f2bf(float f) {
  __hip_bfloat16 h = __float2bfloat16(f);
  return *reinterpret_cast<u16*>(&h);
}
__device__ __forceinline__ bf16x4 pack4(float a, float b, float c, float d) {
  bf16x4 r;
  r[0] = (short)f2bf(a); r[1] = (short)f2bf(b); r[2] = (short)f2bf(c); r[3] = (short)f2bf(d);
  return r;
}
__device__ __forceinline__ void glds16(const void* g, void* l) {
  __builtin_amdgcn_global_load_lds((const unsigned*)g, (unsigned*)l, 16, 0, 0);
}
__device__ __forceinline__ bf16x4 tr_read(const char* p) {
  return __builtin_amdgcn_ds_read_tr16_b64_v4i16((lds_b4_ptr)p);
}
__device__ __forceinline__ bf16x8 cat8(bf16x4 a, bf16x4 b) {
  return (bf16x8){a[0], a[1], a[2], a[3], b[0], b[1], b[2], b[3]};
}
#define WAIT_V(n) asm volatile("s_waitcnt vmcnt(" #n ")" ::: "memory")
#define BAR() __builtin_amdgcn_s_barrier()
template <int CTRL> __device__ __forceinline__ float dppf(float v) {
  return __int_as_float(__builtin_amdgcn_update_dpp(0, __float_as_int(v), CTRL, 0xF, 0xF, true));
}
__device__ __forceinline__ float red16_max(float v) {
  v = fmaxf(v, dppf<0xB1>(v)); v = fmaxf(v, dppf<0x4E>(v));
  v = fmaxf(v, dppf<0x124>(v)); v = fmaxf(v, dppf<0x128>(v)); return v;
}
__device__ __forceinline__ float red16_sum(float v) {
  v += dppf<0xB1>(v); v += dppf<0x4E>(v);
  v += dppf<0x124>(v); v += dppf<0x128>(v); return v;
}

__global__ __launch_bounds__(256) void prep_kernel(const u16* __restrict__ x, u16* __restrict__ xn, float* __restrict__ kvacc) {
  const int wave = (blockIdx.x * 256 + threadIdx.x) >> 6, lane = threadIdx.x & 63;
  const int gid = blockIdx.x * 256 + threadIdx.x;
  if (gid < KV_FLOATS / 4) reinterpret_cast<f32x4*>(kvacc)[gid] = (f32x4){0.f, 0.f, 0.f, 0.f};
  bf16x8 v = *reinterpret_cast<const bf16x8*>(x + (size_t)wave * D + lane * 8);
  float f[8]; float s = 0.f;
#pragma unroll
  for (int j = 0; j < 8; ++j) { f[j] = bf2f((u16)v[j]); s += f[j]; }
#pragma unroll
  for (int o = 32; o > 0; o >>= 1) s += __shfl_xor(s, o);
  const float mean = s * (1.f / D);
  float q = 0.f;
#pragma unroll
  for (int j = 0; j < 8; ++j) { f[j] -= mean; q += f[j] * f[j]; }
#pragma unroll
  for (int o = 32; o > 0; o >>= 1) q += __shfl_xor(q, o);
  const float rstd = rsqrtf(q * (1.f / D) + 1e-5f);
  bf16x8 r;
#pragma unroll
  for (int j = 0; j < 8; ++j) r[j] = (short)f2bf(f[j] * rstd);
  *reinterpret_cast<bf16x8*>(xn + (size_t)wave * D + lane * 8) = r;
}

constexpr int G1_ABYTES = 128 * 64;
constexpr int G1_BBYTES = 32 * 256;
constexpr int G1_STAGE = G1_ABYTES + G1_BBYTES;
constexpr int G1_NS = 4;

template <int TYPE>
__device__ __forceinline__ void gemm1_body(char* smem, int m0, int nt,
                                           const u16* __restrict__ xn, const u16* __restrict__ wqkv,
                                           const u16* __restrict__ wphi, const u16* __restrict__ wg,
                                           const u16* __restrict__ wu, u16* __restrict__ phiq,
                                           u16* __restrict__ hbuf, float* __restrict__ kvacc) {
  const int tid = threadIdx.x, w = tid >> 6, l = tid & 63, wm = w >> 1, wn = w & 1;
  const int g = l >> 4, r16 = l & 15;

  const u16* asrc[2]; const u16* bsrc[2];
  constexpr int LDB = (TYPE == 2) ? F : NQKV;
#pragma unroll
  for (int i = 0; i < 2; ++i) {
    const int row = w * 32 + i * 16 + (l >> 2);
    const int ch = (l & 3) ^ (((row >> 2) & 1) * 3);
    asrc[i] = xn + (size_t)(m0 + row) * D + ch * 8;
    const int krow = w * 8 + i * 4 + g;
    const int bch = r16 ^ (((krow & 3) << 2) | ((krow >> 2) & 3));
    const u16* base;
    if (TYPE == 0) base = wqkv + nt * 128 + bch * 8;
    else if (TYPE == 1) {
      const int ww = bch >> 3, ni = (bch >> 1) & 3, off = (bch & 1) * 8;
      const int h = (nt - 4) * 4 + ww * 2 + (ni & 1);
      base = wqkv + ((ni < 2) ? D : 2 * D) + h * 16 + off;
    } else {
      const int ww = bch >> 3, ni = (bch >> 1) & 3, off = (bch & 1) * 8;
      const int f0 = (nt - 12) * 64 + ww * 32 + (ni & 1) * 16 + off;
      base = ((ni < 2) ? wg : wu) + f0;
    }
    bsrc[i] = base + (size_t)krow * LDB;
  }
  auto stage = [&](int buf, int kt) {
    char* sa = smem + buf * G1_STAGE; char* sb = sa + G1_ABYTES;
#pragma unroll
    for (int i = 0; i < 2; ++i) glds16(asrc[i] + kt * 32, sa + (w * 32 + i * 16) * 64);
#pragma unroll
    for (int i = 0; i < 2; ++i) glds16(bsrc[i] + (size_t)kt * 32 * LDB, sb + (w * 8 + i * 4) * 256);
  };

  f32x4 acc[4][4];
#pragma unroll
  for (int a = 0; a < 4; ++a)
#pragma unroll
    for (int b = 0; b < 4; ++b) acc[a][b] = (f32x4){0.f, 0.f, 0.f, 0.f};

  const int q4 = r16 >> 2, p4 = l & 3;
  auto compute = [&](int buf) {
    const char* sa = smem + buf * G1_STAGE; const char* sb = sa + G1_ABYTES;
    {
      constexpr int kk = 0;
      bf16x8 xa[4], wb[4];
#pragma unroll
      for (int mi = 0; mi < 4; ++mi) {
        const int row = wm * 64 + mi * 16 + r16;
        xa[mi] = *reinterpret_cast<const bf16x8*>(sa + row * 64 + ((g ^ (((row >> 2) & 1) * 3)) << 4));
      }
#pragma unroll
      for (int ni = 0; ni < 4; ++ni) {
        const int c = wn * 4 + ni;
        bf16x4 t[2];
#pragma unroll
        for (int tt = 0; tt < 2; ++tt) {
          const int row = 32 * kk + 8 * g + 4 * tt + q4;
          const int ch = 2 * c + (p4 >> 1);
          t[tt] = tr_read(sb + 256 * row + ((ch ^ (((row & 3) << 2) | ((row >> 2) & 3))) << 4) + 8 * (p4 & 1));
        }
        wb[ni] = cat8(t[0], t[1]);
      }
#pragma unroll
      for (int mi = 0; mi < 4; ++mi)
#pragma unroll
        for (int ni = 0; ni < 4; ++ni) {
          if (TYPE == 1 && ni >= 2) acc[mi][ni] = __builtin_amdgcn_mfma_f32_16x16x32_bf16(xa[mi], wb[ni], acc[mi][ni], 0, 0, 0);
          else acc[mi][ni] = __builtin_amdgcn_mfma_f32_16x16x32_bf16(wb[ni], xa[mi], acc[mi][ni], 0, 0, 0);
        }
    }
  };

  stage(0, 0); stage(1, 1); stage(2, 2);
#pragma unroll 1
  for (int kt = 0; kt < 13; ++kt) {
    WAIT_V(8); BAR();
    stage((kt + 3) & 3, kt + 3);
    compute(kt & 3);
  }
  WAIT_V(8); BAR(); compute(1);
  WAIT_V(4); BAR(); compute(2);
  WAIT_V(0); BAR(); compute(3);

  if (TYPE == 0) {
#pragma unroll
    for (int ni = 0; ni < 4; ++ni) {
      const int h = nt * 8 + wn * 4 + ni;
      bf16x4 wf;
#pragma unroll
      for (int j = 0; j < 4; ++j) wf[j] = (short)wphi[h * 256 + (4 * g + j) * 16 + r16];
#pragma unroll
      for (int mi = 0; mi < 4; ++mi) {
        const bf16x4 qf = pack4(acc[mi][ni][0], acc[mi][ni][1], acc[mi][ni][2], acc[mi][ni][3]);
        f32x4 p = __builtin_amdgcn_mfma_f32_16x16x16bf16_1k(wf, qf, (f32x4){0.f, 0.f, 0.f, 0.f}, 0, 0, 0);
        float mx = fmaxf(fmaxf(p[0], p[1]), fmaxf(p[2], p[3]));
        float mn = fminf(fminf(p[0], p[1]), fminf(p[2], p[3]));
        mx = fmaxf(mx, __shfl_xor(mx, 16)); mx = fmaxf(mx, __shfl_xor(mx, 32));
        mn = fminf(mn, __shfl_xor(mn, 16)); mn = fminf(mn, __shfl_xor(mn, 32));
        float e1[4], e2[4], s1 = 0.f, s2 = 0.f;
#pragma unroll
        for (int j = 0; j < 4; ++j) { e1[j] = __expf(p[j] - mx); e2[j] = __expf(mn - p[j]); s1 += e1[j]; s2 += e2[j]; }
        s1 += __shfl_xor(s1, 16); s1 += __shfl_xor(s1, 32);
        s2 += __shfl_xor(s2, 16); s2 += __shfl_xor(s2, 32);
        const float r1 = 1.f / s1, r2 = 1.f / s2;
        const int tok = m0 + wm * 64 + mi * 16 + r16;
        u16* dst = phiq + (size_t)tok * 1024 + h * 32 + 4 * g;
        *reinterpret_cast<bf16x4*>(dst) = pack4(e1[0] * r1, e1[1] * r1, e1[2] * r1, e1[3] * r1);
        *reinterpret_cast<bf16x4*>(dst + 16) = pack4(e2[0] * r2, e2[1] * r2, e2[2] * r2, e2[3] * r2);
      }
    }
  } else if (TYPE == 1) {
#pragma unroll
    for (int hh = 0; hh < 2; ++hh) {
      const int h = (nt - 4) * 4 + wn * 2 + hh;
      bf16x4 wf;
#pragma unroll
      for (int j = 0; j < 4; ++j) wf[j] = (short)wphi[h * 256 + (4 * g + j) * 16 + r16];
      f32x4 kvp = {0.f, 0.f, 0.f, 0.f}, kvn = {0.f, 0.f, 0.f, 0.f};
      float zp = 0.f, zn = 0.f;
#pragma unroll
      for (int mi = 0; mi < 4; ++mi) {
        const bf16x4 kf = pack4(acc[mi][hh][0], acc[mi][hh][1], acc[mi][hh][2], acc[mi][hh][3]);
        f32x4 p = __builtin_amdgcn_mfma_f32_16x16x16bf16_1k(kf, wf, (f32x4){0.f, 0.f, 0.f, 0.f}, 0, 0, 0);
        float e1[4], e2[4];
#pragma unroll
        for (int j = 0; j < 4; ++j) {
          const float mx = red16_max(p[j]);
          const float mn = -red16_max(-p[j]);
          const float a = __expf(p[j] - mx), b = __expf(mn - p[j]);
          const float s1 = red16_sum(a), s2 = red16_sum(b);
          e1[j] = bf2f(f2bf(a / s1)); e2[j] = bf2f(f2bf(b / s2));
          zp += e1[j]; zn += e2[j];
        }
        const bf16x4 vf = pack4(acc[mi][2 + hh][0], acc[mi][2 + hh][1], acc[mi][2 + hh][2], acc[mi][2 + hh][3]);
        kvp = __builtin_amdgcn_mfma_f32_16x16x16bf16_1k(pack4(e1[0], e1[1], e1[2], e1[3]), vf, kvp, 0, 0, 0);
        kvn = __builtin_amdgcn_mfma_f32_16x16x16bf16_1k(pack4(e2[0], e2[1], e2[2], e2[3]), vf, kvn, 0, 0, 0);
      }
      float* kvh = kvacc + h * 512;
#pragma unroll
      for (int j = 0; j < 4; ++j) {
        atomicAdd(kvh + (4 * g + j) * 16 + r16, kvp[j]);
        atomicAdd(kvh + (16 + 4 * g + j) * 16 + r16, kvn[j]);
      }
      zp += __shfl_xor(zp, 16); zp += __shfl_xor(zp, 32);
      zn += __shfl_xor(zn, 16); zn += __shfl_xor(zn, 32);
      if (l < 16) {
        atomicAdd(kvacc + H * 512 + h * 32 + l, zp);
        atomicAdd(kvacc + H * 512 + h * 32 + 16 + l, zn);
      }
    }
  } else {
#pragma unroll
    for (int ni = 0; ni < 2; ++ni) {
      const int f0 = (nt - 12) * 64 + wn * 32 + ni * 16 + 4 * g;
#pragma unroll
      for (int mi = 0; mi < 4; ++mi) {
        const int tok = m0 + wm * 64 + mi * 16 + r16;
        const f32x4 ga = acc[mi][ni], up = acc[mi][2 + ni];
        *reinterpret_cast<bf16x4*>(hbuf + (size_t)tok * F + f0) =
            pack4(fmaxf(ga[0], 0.f) * up[0], fmaxf(ga[1], 0.f) * up[1], fmaxf(ga[2], 0.f) * up[2], fmaxf(ga[3], 0.f) * up[3]);
      }
    }
  }
}

__global__ __launch_bounds__(256, 2) void gemm1_kernel(const u16* __restrict__ xn, const u16* __restrict__ wqkv,
                                                       const u16* __restrict__ wphi, const u16* __restrict__ wg,
                                                       const u16* __restrict__ wu, u16* __restrict__ phiq,
                                                       u16* __restrict__ hbuf, float* __restrict__ kvacc) {
  __shared__ __attribute__((aligned(16))) char smem[G1_NS * G1_STAGE];
  const int bid = blockIdx.x;
  const int mt = bid & 15, nt = bid >> 4;
  const int m0 = mt * 128;
  if (nt < 4) gemm1_body<0>(smem, m0, nt, xn, wqkv, wphi, wg, wu, phiq, hbuf, kvacc);
  else if (nt < 12) gemm1_body<1>(smem, m0, nt, xn, wqkv, wphi, wg, wu, phiq, hbuf, kvacc);
  else gemm1_body<2>(smem, m0, nt, xn, wqkv, wphi, wg, wu, phiq, hbuf, kvacc);
}

constexpr int G2_OBYTES = 8 * 64 * 128;
constexpr int G2_ABYTES = 64 * 128;
constexpr int G2_BBYTES = 64 * 128;
constexpr int G2_STAGE = G2_ABYTES + G2_BBYTES;
constexpr int G2_SMEM = G2_OBYTES + 4 * G2_STAGE;

__global__ __launch_bounds__(256, 1) void gemm2_kernel(const u16* __restrict__ x, const u16* __restrict__ phiq,
                                                       const u16* __restrict__ hbuf, const float* __restrict__ kvacc,
                                                       const u16* __restrict__ wo, const u16* __restrict__ wd,
                                                       u16* __restrict__ out) {
  extern __shared__ __attribute__((aligned(16))) char smem[];
  char* oimg = smem;
  char* stg = smem + G2_OBYTES;
  const int tid = threadIdx.x, w = tid >> 6, l = tid & 63, g = l >> 4, r16 = l & 15;
  const int bid = blockIdx.x;
  const int mt = bid & 31, nt = bid >> 5;
  const int m0 = mt * 64, n0 = nt * 64;

  const int wk = w >> 1, wn = w & 1;
  int brow[2], bcol[2], arow[2], acol[2];
#pragma unroll
  for (int i = 0; i < 2; ++i) {
    brow[i] = w * 16 + i * 8 + (l >> 3);
    const int s = ((brow[i] >> 1) & 1) | ((brow[i] >> 2) & 2);
    bcol[i] = n0 + (((l & 7) ^ (s << 1)) << 3);
    arow[i] = w * 16 + i * 8 + (l >> 3);
    acol[i] = ((l & 7) ^ ((arow[i] >> 1) & 7)) << 3;
  }
  auto stage = [&](int buf, int st) {
    char* sa = stg + buf * G2_STAGE; char* sb = sa + G2_ABYTES;
    const u16* wsrc = (st < 8) ? (wo + (size_t)st * 64 * D) : (wd + (size_t)(st - 8) * 64 * D);
    const u16* asrc = (st < 8) ? hbuf : (hbuf + (st - 8) * 64);
#pragma unroll
    for (int i = 0; i < 2; ++i) glds16(wsrc + (size_t)brow[i] * D + bcol[i], sb + (w * 16 + i * 8) * 128);
#pragma unroll
    for (int i = 0; i < 2; ++i) glds16(asrc + (size_t)(m0 + arow[i]) * F + acol[i], sa + (w * 16 + i * 8) * 128);
  };
  stage(0, 0); stage(1, 1); stage(2, 2);

#pragma unroll 1
  for (int hh = 0; hh < 8; ++hh) {
    const int h = w * 8 + hh;
    bf16x8 kvf, zf;
#pragma unroll
    for (int j = 0; j < 8; ++j) {
      kvf[j] = (short)f2bf(kvacc[h * 512 + (8 * g + j) * 16 + r16]);
      zf[j] = (short)f2bf(kvacc[H * 512 + h * 32 + 8 * g + j]);
    }
#pragma unroll
    for (int mi = 0; mi < 4; ++mi) {
      const int row = mi * 16 + r16;
      const bf16x8 pf = *reinterpret_cast<const bf16x8*>(phiq + (size_t)(m0 + row) * 1024 + h * 32 + 8 * g);
      f32x4 num = __builtin_amdgcn_mfma_f32_16x16x32_bf16(kvf, pf, (f32x4){0.f, 0.f, 0.f, 0.f}, 0, 0, 0);
      f32x4 den = __builtin_amdgcn_mfma_f32_16x16x32_bf16(zf, pf, (f32x4){0.f, 0.f, 0.f, 0.f}, 0, 0, 0);
      const float rd = 1.f / (den[0] + 1e-6f);
      const int st = h >> 2, ch = (h & 3) * 2 + (g >> 1);
      *reinterpret_cast<bf16x4*>(oimg + st * (64 * 128) + row * 128 + ((ch ^ ((row >> 1) & 7)) << 4) + (g & 1) * 8) =
          pack4(num[0] * rd, num[1] * rd, num[2] * rd, num[3] * rd);
    }
  }
  asm volatile("s_waitcnt lgkmcnt(0)" ::: "memory");

  f32x4 acc[4][2];
#pragma unroll
  for (int a = 0; a < 4; ++a)
#pragma unroll
    for (int b = 0; b < 2; ++b) acc[a][b] = (f32x4){0.f, 0.f, 0.f, 0.f};
  const int q4 = r16 >> 2, p4 = l & 3;
  auto compute = [&](int buf, int st) {
    const char* sa = (st < 8) ? (oimg + st * (64 * 128)) : (stg + buf * G2_STAGE);
    const char* sb = stg + buf * G2_STAGE + G2_ABYTES;
    bf16x8 xa[4], wb[2];
#pragma unroll
    for (int mi = 0; mi < 4; ++mi) {
      const int row = mi * 16 + r16;
      xa[mi] = *reinterpret_cast<const bf16x8*>(sa + row * 128 + (((wk * 4 + g) ^ ((row >> 1) & 7)) << 4));
    }
#pragma unroll
    for (int ni = 0; ni < 2; ++ni) {
      bf16x4 t[2];
#pragma unroll
      for (int tt = 0; tt < 2; ++tt) {
        const int row = 32 * wk + 8 * g + 4 * tt + q4;
        const int s = ((row >> 1) & 1) | ((row >> 2) & 2);
        const int ch = 2 * (wn * 2 + ni) + (p4 >> 1);
        t[tt] = tr_read(sb + 128 * row + ((ch ^ (s << 1)) << 4) + 8 * (p4 & 1));
      }
      wb[ni] = cat8(t[0], t[1]);
    }
#pragma unroll
    for (int mi = 0; mi < 4; ++mi)
#pragma unroll
      for (int ni = 0; ni < 2; ++ni)
        acc[mi][ni] = __builtin_amdgcn_mfma_f32_16x16x32_bf16(wb[ni], xa[mi], acc[mi][ni], 0, 0, 0);
  };

#pragma unroll 1
  for (int st = 0; st < 33; ++st) {
    WAIT_V(8); BAR();
    stage((st + 3) & 3, st + 3);
    compute(st & 3, st);
  }
  WAIT_V(8); BAR(); compute(1, 33);
  WAIT_V(4); BAR(); compute(2, 34);
  WAIT_V(0); BAR(); compute(3, 35);
  __syncthreads();

  float* red = reinterpret_cast<float*>(stg);
#pragma unroll
  for (int mi = 0; mi < 4; ++mi)
#pragma unroll
    for (int ni = 0; ni < 2; ++ni)
      *reinterpret_cast<f32x4*>(red + wk * 4096 + (mi * 16 + r16) * 64 + (wn * 2 + ni) * 16 + 4 * g) = acc[mi][ni];
  __syncthreads();
  const int tok = tid >> 2, nc = (tid & 3) * 16;
  float o[16];
#pragma unroll
  for (int j = 0; j < 4; ++j) {
    f32x4 a = *reinterpret_cast<const f32x4*>(red + tok * 64 + nc + j * 4);
#pragma unroll
    for (int ww = 1; ww < 2; ++ww) {
      f32x4 b = *reinterpret_cast<const f32x4*>(red + ww * 4096 + tok * 64 + nc + j * 4);
      a += b;
    }
    o[j * 4 + 0] = a[0]; o[j * 4 + 1] = a[1]; o[j * 4 + 2] = a[2]; o[j * 4 + 3] = a[3];
  }
  const size_t go = (size_t)(m0 + tok) * D + n0 + nc;
#pragma unroll
  for (int hlf = 0; hlf < 2; ++hlf) {
    const bf16x8 xv = *reinterpret_cast<const bf16x8*>(x + go + hlf * 8);
    bf16x8 r;
#pragma unroll
    for (int j = 0; j < 8; ++j) r[j] = (short)f2bf(bf2f((u16)xv[j]) + o[hlf * 8 + j]);
    *reinterpret_cast<bf16x8*>(out + go + hlf * 8) = r;
  }
}

extern "C" void kernel_launch(void* const* d_in, const int* in_sizes, int n_in,
                              void* d_out, int out_size, void* d_ws, size_t ws_size,
                              hipStream_t stream) {
  const u16* x = (const u16*)d_in[0];
  const u16* wqkv = (const u16*)d_in[1];
  const u16* wphi = (const u16*)d_in[2];
  const u16* wo = (const u16*)d_in[3];
  const u16* wg = (const u16*)d_in[5];
  const u16* wu = (const u16*)d_in[6];
  const u16* wd = (const u16*)d_in[7];
  char* ws = (char*)d_ws;
  u16* xn = (u16*)(ws + WS_XN);
  u16* phiq = (u16*)(ws + WS_PHIQ);
  u16* hbuf = (u16*)(ws + WS_H);
  float* kvacc = (float*)(ws + WS_KV);

  prep_kernel<<<S / 4, 256, 0, stream>>>(x, xn, kvacc);
  gemm1_kernel<<<16 * 40, 256, 0, stream>>>(xn, wqkv, wphi, wg, wu, phiq, hbuf, kvacc);
  static bool attr_set = false;
  if (!attr_set) {
    hipFuncSetAttribute((const void*)gemm2_kernel, hipFuncAttributeMaxDynamicSharedMemorySize, G2_SMEM);
    attr_set = true;
  }
  gemm2_kernel<<<256, 256, G2_SMEM, stream>>>(x, phiq, hbuf, kvacc, wo, wd, (u16*)d_out);
}
```

```cpp
#include <hip/hip_runtime.h>
#include <hip/hip_bf16.h>
#include <stdint.h>

typedef short bf16x8 __attribute__((ext_vector_type(8)));
typedef short bf16x4 __attribute__((ext_vector_type(4)));
typedef float f32x4 __attribute__((ext_vector_type(4)));
typedef unsigned short u16;
typedef __attribute__((address_space(3))) bf16x4* lds_b4_ptr;

constexpr int S = 2048, D = 512, H = 32, F = 1792;
constexpr int NQKV = 3 * D;

constexpr size_t WS_XN = 0;
constexpr size_t WS_PHIQ = WS_XN + (size_t)S * D * 2;
constexpr size_t WS_H = WS_PHIQ + (size_t)S * 1024 * 2;
constexpr size_t WS_CNT = WS_H + (size_t)S * F * 2;
constexpr size_t WS_KVB = WS_CNT + 256;
constexpr size_t WS_PART = WS_KVB + 40960;

__device__ __forceinline__ float bf2f(u16 v) { return __uint_as_float(((unsigned)v) << 16); }
__device__ __forceinline__ u16 f2bf(float f) {
  __hip_bfloat16 h = __float2bfloat16(f);
  return *reinterpret_cast<u16*>(&h);
}
__device__ __forceinline__ bf16x4 pack4(float a, float b, float c, float d) {
  bf16x4 r;
  r[0] = (short)f2bf(a); r[1] = (short)f2bf(b); r[2] = (short)f2bf(c); r[3] = (short)f2bf(d);
  return r;
}
__device__ __forceinline__ void glds16(const void* g, void* l) {
  unsigned keep;
  const unsigned dst = __builtin_amdgcn_readfirstlane((unsigned)(uintptr_t)l);
  asm volatile("s_mov_b32 %0, m0\n\ts_mov_b32 m0, %2\n\ts_nop 0\n\tglobal_load_lds_dwordx4 %1, off\n\ts_mov_b32 m0, %0"
               : "=&s"(keep) : "v"(g), "s"(dst) : "memory");
}
__device__ __forceinline__ bf16x4 tr_read(const char* p) {
  return __builtin_amdgcn_ds_read_tr16_b64_v4i16((lds_b4_ptr)p);
}
__device__ __forceinline__ bf16x8 cat8(bf16x4 a, bf16x4 b) {
  return (bf16x8){a[0], a[1], a[2], a[3], b[0], b[1], b[2], b[3]};
}
#define WAIT_V(n) asm volatile("s_waitcnt vmcnt(" #n ")" ::: "memory")
#define BAR() __builtin_amdgcn_s_barrier()
template <int CTRL> __device__ __forceinline__ float dppf(float v) {
  return __int_as_float(__builtin_amdgcn_update_dpp(0, __float_as_int(v), CTRL, 0xF, 0xF, true));
}
__device__ __forceinline__ float red16_max(float v) {
  v = fmaxf(v, dppf<0xB1>(v)); v = fmaxf(v, dppf<0x4E>(v));
  v = fmaxf(v, dppf<0x124>(v)); v = fmaxf(v, dppf<0x128>(v)); return v;
}
__device__ __forceinline__ float red16_sum(float v) {
  v += dppf<0xB1>(v); v += dppf<0x4E>(v);
  v += dppf<0x124>(v); v += dppf<0x128>(v); return v;
}

__device__ __forceinline__ float xrow_max(float v) {
  auto a = __builtin_amdgcn_permlane16_swap(__float_as_uint(v), __float_as_uint(v), false, false);
  v = fmaxf(__uint_as_float(a[0]), __uint_as_float(a[1]));
  auto b = __builtin_amdgcn_permlane32_swap(__float_as_uint(v), __float_as_uint(v), false, false);
  return fmaxf(__uint_as_float(b[0]), __uint_as_float(b[1]));
}
__device__ __forceinline__ float xrow_sum(float v) {
  auto a = __builtin_amdgcn_permlane16_swap(__float_as_uint(v), __float_as_uint(v), false, false);
  v = __uint_as_float(a[0]) + __uint_as_float(a[1]);
  auto b = __builtin_amdgcn_permlane32_swap(__float_as_uint(v), __float_as_uint(v), false, false);
  return __uint_as_float(b[0]) + __uint_as_float(b[1]);
}

__global__ __launch_bounds__(256) void prep_kernel(const u16* __restrict__ x, u16* __restrict__ xn, unsigned* __restrict__ cnt) {
  const int wave = (blockIdx.x * 256 + threadIdx.x) >> 6, lane = threadIdx.x & 63;
  const int gid = blockIdx.x * 256 + threadIdx.x;
  if (gid < 64) cnt[gid] = 0u;
  bf16x8 v = *reinterpret_cast<const bf16x8*>(x + (size_t)wave * D + lane * 8);
  float f[8]; float s = 0.f;
#pragma unroll
  for (int j = 0; j < 8; ++j) { f[j] = bf2f((u16)v[j]); s += f[j]; }
#pragma unroll
  for (int o = 32; o > 0; o >>= 1) s += __shfl_xor(s, o);
  const float mean = s * (1.f / D);
  float q = 0.f;
#pragma unroll
  for (int j = 0; j < 8; ++j) { f[j] -= mean; q += f[j] * f[j]; }
#pragma unroll
  for (int o = 32; o > 0; o >>= 1) q += __shfl_xor(q, o);
  const float rstd = rsqrtf(q * (1.f / D) + 1e-5f);
  bf16x8 r;
#pragma unroll
  for (int j = 0; j < 8; ++j) r[j] = (short)f2bf(f[j] * rstd);
  *reinterpret_cast<bf16x8*>(xn + (size_t)wave * D + lane * 8) = r;
}

constexpr int G1_ABYTES = 128 * 128;
constexpr int G1_BBYTES = 64 * 320;
constexpr int G1_STAGE = G1_ABYTES + G1_BBYTES;

template <int TYPE>
__device__ __forceinline__ void gemm1_body(char* smem, int mt, int j,
                                           const u16* __restrict__ xn, const u16* __restrict__ wqkv,
                                           const u16* __restrict__ wphi, const u16* __restrict__ wg,
                                           const u16* __restrict__ wu, u16* __restrict__ phiq,
                                           u16* __restrict__ hbuf, float* __restrict__ part, unsigned* __restrict__ cnt,
                                           u16* __restrict__ kvb) {
  const int tid = threadIdx.x, w = tid >> 6, l = tid & 63;
  const int g = l >> 4, r16 = l & 15;
  const int m0 = mt * 128;
  constexpr int NPAIR = (TYPE == 0) ? 4 : 3;
  constexpr int G0 = (TYPE == 0) ? 2 : 4;
  const int f0 = (TYPE == 0) ? j * 64 : 1024 + j * 48;

  const u16* asrc[4]; const u16* bsrc[5]; int bld[5];
#pragma unroll
  for (int i = 0; i < 4; ++i) {
    const int row = w * 32 + i * 8 + (l >> 3);
    asrc[i] = xn + (size_t)(m0 + row) * D + (((l & 7) ^ ((row >> 1) & 7)) << 3);
  }
#pragma unroll
  for (int i = 0; i < 5; ++i) {
    const int c = (w * 5 + i) * 64 + l, row = c / 20, chs = c - row * 20;
    const int ch = chs ^ (((row >> 3) & 1) << 1);
    const int ni = ch >> 1, off = (ch & 1) * 8;
    const u16* base; int ld;
    if (ni >= G0) { base = (((ni - G0) & 1) ? wu : wg) + f0 + ((ni - G0) >> 1) * 16 + off; ld = F; }
    else if (TYPE == 0) { base = wqkv + (2 * j + ni) * 16 + off; ld = NQKV; }
    else { base = wqkv + ((ni < 2) ? D : 2 * D) + (2 * j + (ni & 1)) * 16 + off; ld = NQKV; }
    bsrc[i] = base + (size_t)row * ld; bld[i] = 64 * ld;
  }
  auto stage = [&](int buf, int kt) {
    char* sa = smem + buf * G1_STAGE; char* sb = sa + G1_ABYTES;
#pragma unroll
    for (int i = 0; i < 4; ++i) glds16(asrc[i] + kt * 64, sa + (w * 32 + i * 8) * 128);
#pragma unroll
    for (int i = 0; i < 5; ++i) glds16(bsrc[i] + (size_t)kt * bld[i], sb + (w * 5 + i) * 1024);
  };

  f32x4 acc[2][10];
#pragma unroll
  for (int a = 0; a < 2; ++a)
#pragma unroll
    for (int b = 0; b < 10; ++b) acc[a][b] = (f32x4){0.f, 0.f, 0.f, 0.f};

  const int q4 = r16 >> 2, p4 = l & 3;
  auto compute = [&](int buf) {
    const char* sa = smem + buf * G1_STAGE; const char* sb = sa + G1_ABYTES;
#pragma unroll
    for (int kk = 0; kk < 2; ++kk) {
      bf16x8 xa[2];
#pragma unroll
      for (int mi = 0; mi < 2; ++mi) {
        const int row = w * 32 + mi * 16 + r16;
        xa[mi] = *reinterpret_cast<const bf16x8*>(sa + row * 128 + (((kk * 4 + g) ^ ((row >> 1) & 7)) << 4));
      }
#pragma unroll
      for (int ni = 0; ni < 10; ++ni) {
        bf16x4 t[2];
#pragma unroll
        for (int tt = 0; tt < 2; ++tt) {
          const int row = 32 * kk + 8 * g + 4 * tt + q4;
          const int ch = 2 * ni + (p4 >> 1);
          t[tt] = tr_read(sb + 320 * row + ((ch ^ (((row >> 3) & 1) << 1)) << 4) + 8 * (p4 & 1));
        }
        const bf16x8 wb = cat8(t[0], t[1]);
#pragma unroll
        for (int mi = 0; mi < 2; ++mi) {
          if (TYPE == 1 && (ni == 2 || ni == 3)) acc[mi][ni] = __builtin_amdgcn_mfma_f32_16x16x32_bf16(xa[mi], wb, acc[mi][ni], 0, 0, 0);
          else acc[mi][ni] = __builtin_amdgcn_mfma_f32_16x16x32_bf16(wb, xa[mi], acc[mi][ni], 0, 0, 0);
        }
      }
    }
  };

  stage(0, 0);
#pragma unroll 1
  for (int kt = 0; kt < 7; ++kt) {
    WAIT_V(0); BAR();
    stage((kt + 1) & 1, kt + 1);
    compute(kt & 1);
  }
  WAIT_V(0); BAR(); compute(1);
  asm volatile("s_waitcnt lgkmcnt(0)" ::: "memory");
  BAR();

  char* stgw = smem + w * 9216;
  {
#pragma unroll
    for (int pp = 0; pp < NPAIR; ++pp)
#pragma unroll
      for (int mi = 0; mi < 2; ++mi) {
        const f32x4 ga = acc[mi][G0 + 2 * pp], up = acc[mi][G0 + 2 * pp + 1];
        *reinterpret_cast<bf16x4*>(stgw + (mi * 16 + r16) * 144 + pp * 32 + g * 8) =
            pack4(fmaxf(ga[0], 0.f) * up[0], fmaxf(ga[1], 0.f) * up[1], fmaxf(ga[2], 0.f) * up[2], fmaxf(ga[3], 0.f) * up[3]);
      }
  }
  bf16x4 wf[2];
#pragma unroll
  for (int hh = 0; hh < 2; ++hh)
#pragma unroll
    for (int jj = 0; jj < 4; ++jj) wf[hh][jj] = (short)wphi[(2 * j + hh) * 256 + (4 * g + jj) * 16 + r16];

  if (TYPE == 0) {
#pragma unroll
    for (int hh = 0; hh < 2; ++hh) {
      f32x4 p[2]; float mx[2], mn[2], s1[2], s2[2];
#pragma unroll
      for (int mi = 0; mi < 2; ++mi) {
        const bf16x4 qf = pack4(acc[mi][hh][0], acc[mi][hh][1], acc[mi][hh][2], acc[mi][hh][3]);
        p[mi] = __builtin_amdgcn_mfma_f32_16x16x16bf16_1k(wf[hh], qf, (f32x4){0.f, 0.f, 0.f, 0.f}, 0, 0, 0);
      }
#pragma unroll
      for (int mi = 0; mi < 2; ++mi) {
        mx[mi] = xrow_max(fmaxf(fmaxf(p[mi][0], p[mi][1]), fmaxf(p[mi][2], p[mi][3])));
        mn[mi] = -xrow_max(-fminf(fminf(p[mi][0], p[mi][1]), fminf(p[mi][2], p[mi][3])));
      }
      f32x4 e1[2], e2[2];
#pragma unroll
      for (int mi = 0; mi < 2; ++mi) {
#pragma unroll
        for (int jj = 0; jj < 4; ++jj) { e1[mi][jj] = __expf(p[mi][jj] - mx[mi]); e2[mi][jj] = __expf(mn[mi] - p[mi][jj]); }
        s1[mi] = xrow_sum(e1[mi][0] + e1[mi][1] + e1[mi][2] + e1[mi][3]);
        s2[mi] = xrow_sum(e2[mi][0] + e2[mi][1] + e2[mi][2] + e2[mi][3]);
      }
#pragma unroll
      for (int mi = 0; mi < 2; ++mi) {
        const float r1 = __builtin_amdgcn_rcpf(s1[mi]), r2 = __builtin_amdgcn_rcpf(s2[mi]);
        char* dst = stgw + 4608 + (mi * 16 + r16) * 144 + hh * 64 + g * 8;
        *reinterpret_cast<bf16x4*>(dst) = pack4(e1[mi][0] * r1, e1[mi][1] * r1, e1[mi][2] * r1, e1[mi][3] * r1);
        *reinterpret_cast<bf16x4*>(dst + 32) = pack4(e2[mi][0] * r2, e2[mi][1] * r2, e2[mi][2] * r2, e2[mi][3] * r2);
      }
    }
    asm volatile("s_waitcnt lgkmcnt(0)" ::: "memory");
    __builtin_amdgcn_wave_barrier();
#pragma unroll
    for (int i = 0; i < 4; ++i) {
      const int tk = i * 8 + (l >> 3), ch = l & 7;
      const bf16x8 v = *reinterpret_cast<const bf16x8*>(stgw + tk * 144 + ch * 16);
      *reinterpret_cast<bf16x8*>(hbuf + (size_t)(m0 + w * 32 + tk) * F + f0 + ch * 8) = v;
      const bf16x8 v2 = *reinterpret_cast<const bf16x8*>(stgw + 4608 + tk * 144 + ch * 16);
      *reinterpret_cast<bf16x8*>(phiq + (size_t)(m0 + w * 32 + tk) * 1024 + j * 64 + ch * 8) = v2;
    }
  } else {
    asm volatile("s_waitcnt lgkmcnt(0)" ::: "memory");
    __builtin_amdgcn_wave_barrier();
#pragma unroll
    for (int i = 0; i < 3; ++i) {
      const int idx = i * 64 + l, tk = idx / 6, ch = idx - tk * 6;
      const bf16x8 v = *reinterpret_cast<const bf16x8*>(stgw + tk * 144 + ch * 16);
      *reinterpret_cast<bf16x8*>(hbuf + (size_t)(m0 + w * 32 + tk) * F + f0 + ch * 8) = v;
    }
    float* red = reinterpret_cast<float*>(smem + 4 * 9216);
#pragma unroll
    for (int hh = 0; hh < 2; ++hh) {
      f32x4 p[2];
#pragma unroll
      for (int mi = 0; mi < 2; ++mi) {
        const bf16x4 kf = pack4(acc[mi][hh][0], acc[mi][hh][1], acc[mi][hh][2], acc[mi][hh][3]);
        p[mi] = __builtin_amdgcn_mfma_f32_16x16x16bf16_1k(kf, wf[hh], (f32x4){0.f, 0.f, 0.f, 0.f}, 0, 0, 0);
      }
      f32x4 mx[2], mn[2], e1[2], e2[2];
#pragma unroll
      for (int mi = 0; mi < 2; ++mi)
#pragma unroll
        for (int jj = 0; jj < 4; ++jj) { mx[mi][jj] = red16_max(p[mi][jj]); mn[mi][jj] = -red16_max(-p[mi][jj]); }
#pragma unroll
      for (int mi = 0; mi < 2; ++mi)
#pragma unroll
        for (int jj = 0; jj < 4; ++jj) { e1[mi][jj] = __expf(p[mi][jj] - mx[mi][jj]); e2[mi][jj] = __expf(mn[mi][jj] - p[mi][jj]); }
      float zp = 0.f, zn = 0.f;
#pragma unroll
      for (int mi = 0; mi < 2; ++mi)
#pragma unroll
        for (int jj = 0; jj < 4; ++jj) {
          const float s1 = red16_sum(e1[mi][jj]), s2 = red16_sum(e2[mi][jj]);
          e1[mi][jj] = bf2f(f2bf(e1[mi][jj] * __builtin_amdgcn_rcpf(s1)));
          e2[mi][jj] = bf2f(f2bf(e2[mi][jj] * __builtin_amdgcn_rcpf(s2)));
          zp += e1[mi][jj]; zn += e2[mi][jj];
        }
      f32x4 kvp = {0.f, 0.f, 0.f, 0.f}, kvn = {0.f, 0.f, 0.f, 0.f};
#pragma unroll
      for (int mi = 0; mi < 2; ++mi) {
        const bf16x4 vf = pack4(acc[mi][2 + hh][0], acc[mi][2 + hh][1], acc[mi][2 + hh][2], acc[mi][2 + hh][3]);
        kvp = __builtin_amdgcn_mfma_f32_16x16x16bf16_1k(pack4(e1[mi][0], e1[mi][1], e1[mi][2], e1[mi][3]), vf, kvp, 0, 0, 0);
        kvn = __builtin_amdgcn_mfma_f32_16x16x16bf16_1k(pack4(e2[mi][0], e2[mi][1], e2[mi][2], e2[mi][3]), vf, kvn, 0, 0, 0);
      }
      zp = xrow_sum(zp); zn = xrow_sum(zn);
      float* rw = red + w * 1088 + hh * 544;
#pragma unroll
      for (int jj = 0; jj < 4; ++jj) {
        rw[(4 * g + jj) * 16 + r16] = kvp[jj];
        rw[(16 + 4 * g + jj) * 16 + r16] = kvn[jj];
      }
      if (l < 16) { rw[512 + l] = zp; rw[528 + l] = zn; }
    }
    __syncthreads();
    float* pdst = part + (size_t)(j * 16 + mt) * 1088;
    for (int idx = tid; idx < 272; idx += 256) {
      f32x4 v = *reinterpret_cast<const f32x4*>(red + idx * 4);
#pragma unroll
      for (int ww = 1; ww < 4; ++ww) v += *reinterpret_cast<const f32x4*>(red + ww * 1088 + idx * 4);
      *reinterpret_cast<f32x4*>(pdst + idx * 4) = v;
    }
    asm volatile("s_waitcnt vmcnt(0)" ::: "memory");
    __syncthreads();
    unsigned* flag = reinterpret_cast<unsigned*>(smem + 4 * 9216 + 4 * 1088 * 4);
    if (tid == 0) {
      __builtin_amdgcn_fence(__ATOMIC_RELEASE, "agent");
      asm volatile("s_waitcnt vmcnt(0)" ::: "memory");
      const unsigned old = __hip_atomic_fetch_add(cnt + j, 1u, __ATOMIC_RELAXED, __HIP_MEMORY_SCOPE_AGENT);
      if (old == 15u) { __builtin_amdgcn_fence(__ATOMIC_ACQUIRE, "agent"); asm volatile("s_waitcnt vmcnt(0)" ::: "memory"); }
      *flag = old;
    }
    __syncthreads();
    if (*flag == 15u) {
      const float* psrc = part + (size_t)j * 16 * 1088;
      for (int idx = tid; idx < 272; idx += 256) {
        f32x4 v = {0.f, 0.f, 0.f, 0.f};
#pragma unroll
        for (int m = 0; m < 16; ++m) v += *reinterpret_cast<const f32x4*>(psrc + m * 1088 + idx * 4);
        const int hh = idx >= 136, e = idx * 4 - hh * 544, h = 2 * j + hh;
        if (e < 512) {
          const int f = e >> 4, d = e & 15;
#pragma unroll
          for (int q = 0; q < 4; ++q) kvb[h * 512 + (d + q) * 32 + f] = f2bf(v[q]);
        } else {
          *reinterpret_cast<bf16x4*>(kvb + H * 512 + h * 32 + (e - 512)) = pack4(v[0], v[1], v[2], v[3]);
        }
      }
    }
  }
}

__global__ __launch_bounds__(256, 2) void gemm1_kernel(const u16* __restrict__ xn, const u16* __restrict__ wqkv,
                                                       const u16* __restrict__ wphi, const u16* __restrict__ wg,
                                                       const u16* __restrict__ wu, u16* __restrict__ phiq,
                                                       u16* __restrict__ hbuf, float* __restrict__ part, unsigned* __restrict__ cnt,
                                                       u16* __restrict__ kvb) {
  __shared__ __attribute__((aligned(16))) char smem[2 * G1_STAGE];
  const int bid = blockIdx.x;
  const int xcd = bid & 7, idx = bid >> 3;
  const int mt = (xcd & 1) * 8 + (idx & 7), cb = (xcd >> 1) * 8 + (idx >> 3);
  if (cb & 1) gemm1_body<1>(smem, mt, cb >> 1, xn, wqkv, wphi, wg, wu, phiq, hbuf, part, cnt, kvb);
  else gemm1_body<0>(smem, mt, cb >> 1, xn, wqkv, wphi, wg, wu, phiq, hbuf, part, cnt, kvb);
}

constexpr int G2_OBYTES = 8 * 64 * 128;
constexpr int G2_ABYTES = 64 * 128;
constexpr int G2_BBYTES = 64 * 128;
constexpr int G2_STAGE = G2_ABYTES + G2_BBYTES;
constexpr int G2_SMEM = G2_OBYTES + 4 * G2_STAGE;

__global__ __launch_bounds__(256, 1) void gemm2_kernel(const u16* __restrict__ x, const u16* __restrict__ phiq,
                                                       const u16* __restrict__ hbuf, const u16* __restrict__ kvb,
                                                       const u16* __restrict__ wo, const u16* __restrict__ wd,
                                                       u16* __restrict__ out) {
  __shared__ __attribute__((aligned(16))) char smem[G2_SMEM];
  char* oimg = smem;
  char* stg = smem + G2_OBYTES;
  const int tid = threadIdx.x, w = tid >> 6, l = tid & 63, g = l >> 4, r16 = l & 15;
  const int bid = blockIdx.x;
  const int mt = bid & 31, nt = bid >> 5;
  const int m0 = mt * 64, n0 = nt * 64;

  const int wk = w >> 1, wn = w & 1;
  int brow[2], bcol[2], arow[2], acol[2];
#pragma unroll
  for (int i = 0; i < 2; ++i) {
    brow[i] = w * 16 + i * 8 + (l >> 3);
    const int s = ((brow[i] >> 1) & 1) | ((brow[i] >> 2) & 2);
    bcol[i] = n0 + (((l & 7) ^ (s << 1)) << 3);
    arow[i] = w * 16 + i * 8 + (l >> 3);
    acol[i] = ((l & 7) ^ ((arow[i] >> 1) & 7)) << 3;
  }
  auto stage = [&](int buf, int st) {
    char* sa = stg + buf * G2_STAGE; char* sb = sa + G2_ABYTES;
    const u16* wsrc = (st < 8) ? (wo + (size_t)st * 64 * D) : (wd + (size_t)(st - 8) * 64 * D);
    const u16* asrc = (st < 8) ? hbuf : (hbuf + (st - 8) * 64);
#pragma unroll
    for (int i = 0; i < 2; ++i) glds16(wsrc + (size_t)brow[i] * D + bcol[i], sb + (w * 16 + i * 8) * 128);
#pragma unroll
    for (int i = 0; i < 2; ++i) glds16(asrc + (size_t)(m0 + arow[i]) * F + acol[i], sa + (w * 16 + i * 8) * 128);
  };
  stage(0, 0); stage(1, 1); stage(2, 2);

#pragma unroll 1
  for (int hh = 0; hh < 8; ++hh) {
    const int h = w * 8 + hh;
    const bf16x8 kvf = *reinterpret_cast<const bf16x8*>(kvb + h * 512 + r16 * 32 + 8 * g);
    const bf16x8 zf = *reinterpret_cast<const bf16x8*>(kvb + H * 512 + h * 32 + 8 * g);
#pragma unroll
    for (int mi = 0; mi < 4; ++mi) {
      const int row = mi * 16 + r16;
      const bf16x8 pf = *reinterpret_cast<const bf16x8*>(phiq + (size_t)(m0 + row) * 1024 + h * 32 + 8 * g);
      f32x4 num = __builtin_amdgcn_mfma_f32_16x16x32_bf16(kvf, pf, (f32x4){0.f, 0.f, 0.f, 0.f}, 0, 0, 0);
      f32x4 den = __builtin_amdgcn_mfma_f32_16x16x32_bf16(zf, pf, (f32x4){0.f, 0.f, 0.f, 0.f}, 0, 0, 0);
      const float rd = 1.f / (den[0] + 1e-6f);
      const int st = h >> 2, ch = (h & 3) * 2 + (g >> 1);
      *reinterpret_cast<bf16x4*>(oimg + st * (64 * 128) + row * 128 + ((ch ^ ((row >> 1) & 7)) << 4) + (g & 1) * 8) =
          pack4(num[0] * rd, num[1] * rd, num[2] * rd, num[3] * rd);
    }
  }
  asm volatile("s_waitcnt lgkmcnt(0)" ::: "memory");

  f32x4 acc[4][2];
#pragma unroll
  for (int a = 0; a < 4; ++a)
#pragma unroll
    for (int b = 0; b < 2; ++b) acc[a][b] = (f32x4){0.f, 0.f, 0.f, 0.f};
  const int q4 = r16 >> 2, p4 = l & 3;
  auto compute = [&](int buf, int st) {
    const char* sa = (st < 8) ? (oimg + st * (64 * 128)) : (stg + buf * G2_STAGE);
    const char* sb = stg + buf * G2_STAGE + G2_ABYTES;
    bf16x8 xa[4], wb[2];
#pragma unroll
    for (int mi = 0; mi < 4; ++mi) {
      const int row = mi * 16 + r16;
      xa[mi] = *reinterpret_cast<const bf16x8*>(sa + row * 128 + (((wk * 4 + g) ^ ((row >> 1) & 7)) << 4));
    }
#pragma unroll
    for (int ni = 0; ni < 2; ++ni) {
      bf16x4 t[2];
#pragma unroll
      for (int tt = 0; tt < 2; ++tt) {
        const int row = 32 * wk + 8 * g + 4 * tt + q4;
        const int s = ((row >> 1) & 1) | ((row >> 2) & 2);
        const int ch = 2 * (wn * 2 + ni) + (p4 >> 1);
        t[tt] = tr_read(sb + 128 * row + ((ch ^ (s << 1)) << 4) + 8 * (p4 & 1));
      }
      wb[ni] = cat8(t[0], t[1]);
    }
#pragma unroll
    for (int mi = 0; mi < 4; ++mi)
#pragma unroll
      for (int ni = 0; ni < 2; ++ni)
        acc[mi][ni] = __builtin_amdgcn_mfma_f32_16x16x32_bf16(wb[ni], xa[mi], acc[mi][ni], 0, 0, 0);
  };

#pragma unroll 1
  for (int st = 0; st < 33; ++st) {
    WAIT_V(8); BAR();
    stage((st + 3) & 3, st + 3);
    compute(st & 3, st);
  }
  WAIT_V(8); BAR(); compute(1, 33);
  WAIT_V(4); BAR(); compute(2, 34);
  WAIT_V(0); BAR(); compute(3, 35);
  __syncthreads();

  float* red = reinterpret_cast<float*>(stg);
#pragma unroll
  for (int mi = 0; mi < 4; ++mi)
#pragma unroll
    for (int ni = 0; ni < 2; ++ni)
      *reinterpret_cast<f32x4*>(red + wk * 4096 + (mi * 16 + r16) * 64 + (wn * 2 + ni) * 16 + 4 * g) = acc[mi][ni];
  __syncthreads();
  const int tok = tid >> 2, nc = (tid & 3) * 16;
  float o[16];
#pragma unroll
  for (int j = 0; j < 4; ++j) {
    f32x4 a = *reinterpret_cast<const f32x4*>(red + tok * 64 + nc + j * 4);
#pragma unroll
    for (int ww = 1; ww < 2; ++ww) {
      f32x4 b = *reinterpret_cast<const f32x4*>(red + ww * 4096 + tok * 64 + nc + j * 4);
      a += b;
    }
    o[j * 4 + 0] = a[0]; o[j * 4 + 1] = a[1]; o[j * 4 + 2] = a[2]; o[j * 4 + 3] = a[3];
  }
  const size_t go = (size_t)(m0 + tok) * D + n0 + nc;
#pragma unroll
  for (int hlf = 0; hlf < 2; ++hlf) {
    const bf16x8 xv = *reinterpret_cast<const bf16x8*>(x + go + hlf * 8);
    bf16x8 r;
#pragma unroll
    for (int j = 0; j < 8; ++j) r[j] = (short)f2bf(bf2f((u16)xv[j]) + o[hlf * 8 + j]);
    *reinterpret_cast<bf16x8*>(out + go + hlf * 8) = r;
  }
}

extern "C" void kernel_launch(void* const* d_in, const int* in_sizes, int n_in,
                              void* d_out, int out_size, void* d_ws, size_t ws_size,
                              hipStream_t stream) {
  const u16* x = (const u16*)d_in[0];
  const u16* wqkv = (const u16*)d_in[1];
  const u16* wphi = (const u16*)d_in[2];
  const u16* wo = (const u16*)d_in[3];
  const u16* wg = (const u16*)d_in[5];
  const u16* wu = (const u16*)d_in[6];
  const u16* wd = (const u16*)d_in[7];
  char* ws = (char*)d_ws;
  u16* xn = (u16*)(ws + WS_XN);
  u16* phiq = (u16*)(ws + WS_PHIQ);
  u16* hbuf = (u16*)(ws + WS_H);
  unsigned* cnt = (unsigned*)(ws + WS_CNT);
  u16* kvb = (u16*)(ws + WS_KVB);
  float* part = (float*)(ws + WS_PART);

  prep_kernel<<<S / 4, 256, 0, stream>>>(x, xn, cnt);
  gemm1_kernel<<<512, 256, 0, stream>>>(xn, wqkv, wphi, wg, wu, phiq, hbuf, part, cnt, kvb);
  static bool attr_set = false;
  if (!attr_set) {
    hipFuncSetAttribute((const void*)gemm2_kernel, hipFuncAttributeMaxDynamicSharedMemorySize, G2_SMEM);
    attr_set = true;
  }
  gemm2_kernel<<<256, 256, 0, stream>>>(x, phiq, hbuf, kvb, wo, wd, (u16*)d_out);
}
```

```cpp
#include <hip/hip_runtime.h>
#include <hip/hip_bf16.h>
#include <stdint.h>

typedef short bf16x8 __attribute__((ext_vector_type(8)));
typedef short bf16x4 __attribute__((ext_vector_type(4)));
typedef float f32x4 __attribute__((ext_vector_type(4)));
typedef unsigned short u16;
typedef __attribute__((address_space(3))) bf16x4* lds_b4_ptr;

constexpr int S = 2048, D = 512, H = 32, F = 1792;
constexpr int NQKV = 3 * D;

constexpr size_t WS_XN = 0;
constexpr size_t WS_PHIQ = WS_XN + (size_t)S * D * 2;
constexpr size_t WS_H = WS_PHIQ + (size_t)S * 1024 * 2;
constexpr size_t WS_CNT = WS_H + (size_t)S * F * 2;
constexpr size_t WS_KVB = WS_CNT + 256;
constexpr size_t WS_PART = WS_KVB + 40960;

__device__ __forceinline__ float bf2f(u16 v) { return __uint_as_float(((unsigned)v) << 16); }
__device__ __forceinline__ u16 f2bf(float f) {
  __hip_bfloat16 h = __float2bfloat16(f);
  return *reinterpret_cast<u16*>(&h);
}
__device__ __forceinline__ bf16x4 pack4(float a, float b, float c, float d) {
  bf16x4 r;
  r[0] = (short)f2bf(a); r[1] = (short)f2bf(b); r[2] = (short)f2bf(c); r[3] = (short)f2bf(d);
  return r;
}
__device__ __forceinline__ void glds16(const void* g, void* l) {
  unsigned keep;
  const unsigned dst = __builtin_amdgcn_readfirstlane((unsigned)(uintptr_t)l);
  asm volatile("s_mov_b32 %0, m0\n\ts_mov_b32 m0, %2\n\ts_nop 0\n\tglobal_load_lds_dwordx4 %1, off\n\ts_mov_b32 m0, %0"
               : "=&s"(keep) : "v"(g), "s"(dst) : "memory");
}
__device__ __forceinline__ bf16x4 tr_read(const char* p) {
  return __builtin_amdgcn_ds_read_tr16_b64_v4i16((lds_b4_ptr)p);
}
__device__ __forceinline__ bf16x8 cat8(bf16x4 a, bf16x4 b) {
  return (bf16x8){a[0], a[1], a[2], a[3], b[0], b[1], b[2], b[3]};
}
#define WAIT_V(n) asm volatile("s_waitcnt vmcnt(" #n ")" ::: "memory")
#define BAR() __builtin_amdgcn_s_barrier()
template <int CTRL> __device__ __forceinline__ float dppf(float v) {
  return __int_as_float(__builtin_amdgcn_update_dpp(0, __float_as_int(v), CTRL, 0xF, 0xF, true));
}
__device__ __forceinline__ float red16_max(float v) {
  v = fmaxf(v, dppf<0xB1>(v)); v = fmaxf(v, dppf<0x4E>(v));
  v = fmaxf(v, dppf<0x124>(v)); v = fmaxf(v, dppf<0x128>(v)); return v;
}
__device__ __forceinline__ float red16_sum(float v) {
  v += dppf<0xB1>(v); v += dppf<0x4E>(v);
  v += dppf<0x124>(v); v += dppf<0x128>(v); return v;
}

__device__ __forceinline__ float xrow_max(float v) {
  auto a = __builtin_amdgcn_permlane16_swap(__float_as_uint(v), __float_as_uint(v), false, false);
  v = fmaxf(__uint_as_float(a[0]), __uint_as_float(a[1]));
  auto b = __builtin_amdgcn_permlane32_swap(__float_as_uint(v), __float_as_uint(v), false, false);
  return fmaxf(__uint_as_float(b[0]), __uint_as_float(b[1]));
}
__device__ __forceinline__ float xrow_sum(float v) {
  auto a = __builtin_amdgcn_permlane16_swap(__float_as_uint(v), __float_as_uint(v), false, false);
  v = __uint_as_float(a[0]) + __uint_as_float(a[1]);
  auto b = __builtin_amdgcn_permlane32_swap(__float_as_uint(v), __float_as_uint(v), false, false);
  return __uint_as_float(b[0]) + __uint_as_float(b[1]);
}

__global__ __launch_bounds__(256) void prep_kernel(const u16* __restrict__ x, u16* __restrict__ xn, unsigned* __restrict__ cnt) {
  const int wave = (blockIdx.x * 256 + threadIdx.x) >> 6, lane = threadIdx.x & 63;
  const int gid = blockIdx.x * 256 + threadIdx.x;
  if (gid < 64) cnt[gid] = 0u;
  bf16x8 v = *reinterpret_cast<const bf16x8*>(x + (size_t)wave * D + lane * 8);
  float f[8]; float s = 0.f;
#pragma unroll
  for (int j = 0; j < 8; ++j) { f[j] = bf2f((u16)v[j]); s += f[j]; }
#pragma unroll
  for (int o = 32; o > 0; o >>= 1) s += __shfl_xor(s, o);
  const float mean = s * (1.f / D);
  float q = 0.f;
#pragma unroll
  for (int j = 0; j < 8; ++j) { f[j] -= mean; q += f[j] * f[j]; }
#pragma unroll
  for (int o = 32; o > 0; o >>= 1) q += __shfl_xor(q, o);
  const float rstd = rsqrtf(q * (1.f / D) + 1e-5f);
  bf16x8 r;
#pragma unroll
  for (int j = 0; j < 8; ++j) r[j] = (short)f2bf(f[j] * rstd);
  *reinterpret_cast<bf16x8*>(xn + (size_t)wave * D + lane * 8) = r;
}

constexpr int G1_ABYTES = 128 * 128;
constexpr int G1_BBYTES = 64 * 320;
constexpr int G1_STAGE = G1_ABYTES + G1_BBYTES;

template <int TYPE>
__device__ __forceinline__ void gemm1_body(char* smem, int mt, int j,
                                           const u16* __restrict__ xn, const u16* __restrict__ wqkv,
                                           const u16* __restrict__ wphi, const u16* __restrict__ wg,
                                           const u16* __restrict__ wu, u16* __restrict__ phiq,
                                           u16* __restrict__ hbuf, float* __restrict__ part, unsigned* __restrict__ cnt,
                                           u16* __restrict__ kvb) {
  const int tid = threadIdx.x, w = tid >> 6, l = tid & 63;
  const int g = l >> 4, r16 = l & 15;
  const int m0 = mt * 128;
  constexpr int NPAIR = (TYPE == 0) ? 4 : 3;
  constexpr int G0 = (TYPE == 0) ? 2 : 4;
  const int f0 = (TYPE == 0) ? j * 64 : 1024 + j * 48;

  const u16* asrc[4]; const u16* bsrc[5]; int bld[5];
#pragma unroll
  for (int i = 0; i < 4; ++i) {
    const int row = w * 32 + i * 8 + (l >> 3);
    asrc[i] = xn + (size_t)(m0 + row) * D + (((l & 7) ^ ((row >> 1) & 7)) << 3);
  }
#pragma unroll
  for (int i = 0; i < 5; ++i) {
    const int c = (w * 5 + i) * 64 + l, row = c / 20, chs = c - row * 20;
    const int ch = chs ^ (((row >> 3) & 1) << 1);
    const int ni = ch >> 1, off = (ch & 1) * 8;
    const u16* base; int ld;
    if (ni >= G0) { base = (((ni - G0) & 1) ? wu : wg) + f0 + ((ni - G0) >> 1) * 16 + off; ld = F; }
    else if (TYPE == 0) { base = wqkv + (2 * j + ni) * 16 + off; ld = NQKV; }
    else { base = wqkv + ((ni < 2) ? D : 2 * D) + (2 * j + (ni & 1)) * 16 + off; ld = NQKV; }
    bsrc[i] = base + (size_t)row * ld; bld[i] = 64 * ld;
  }
  auto stage = [&](int buf, int kt) {
    char* sa = smem + buf * G1_STAGE; char* sb = sa + G1_ABYTES;
#pragma unroll
    for (int i = 0; i < 4; ++i) glds16(asrc[i] + kt * 64, sa + (w * 32 + i * 8) * 128);
#pragma unroll
    for (int i = 0; i < 5; ++i) glds16(bsrc[i] + (size_t)kt * bld[i], sb + (w * 5 + i) * 1024);
  };

  f32x4 acc[2][10];
#pragma unroll
  for (int a = 0; a < 2; ++a)
#pragma unroll
    for (int b = 0; b < 10; ++b) acc[a][b] = (f32x4){0.f, 0.f, 0.f, 0.f};

  const int q4 = r16 >> 2, p4 = l & 3;
  auto compute = [&](int buf) {
    const char* sa = smem + buf * G1_STAGE; const char* sb = sa + G1_ABYTES;
#pragma unroll
    for (int kk = 0; kk < 2; ++kk) {
      bf16x8 xa[2];
#pragma unroll
      for (int mi = 0; mi < 2; ++mi) {
        const int row = w * 32 + mi * 16 + r16;
        xa[mi] = *reinterpret_cast<const bf16x8*>(sa + row * 128 + (((kk * 4 + g) ^ ((row >> 1) & 7)) << 4));
      }
#pragma unroll
      for (int ni = 0; ni < 10; ++ni) {
        bf16x4 t[2];
#pragma unroll
        for (int tt = 0; tt < 2; ++tt) {
          const int row = 32 * kk + 8 * g + 4 * tt + q4;
          const int ch = 2 * ni + (p4 >> 1);
          t[tt] = tr_read(sb + 320 * row + ((ch ^ (((row >> 3) & 1) << 1)) << 4) + 8 * (p4 & 1));
        }
        const bf16x8 wb = cat8(t[0], t[1]);
#pragma unroll
        for (int mi = 0; mi < 2; ++mi) {
          if (TYPE == 1 && (ni == 2 || ni == 3)) acc[mi][ni] = __builtin_amdgcn_mfma_f32_16x16x32_bf16(xa[mi], wb, acc[mi][ni], 0, 0, 0);
          else acc[mi][ni] = __builtin_amdgcn_mfma_f32_16x16x32_bf16(wb, xa[mi], acc[mi][ni], 0, 0, 0);
        }
      }
    }
  };

  stage(0, 0);
#pragma unroll 1
  for (int kt = 0; kt < 7; ++kt) {
    WAIT_V(0); BAR();
    stage((kt + 1) & 1, kt + 1);
    compute(kt & 1);
  }
  WAIT_V(0); BAR(); compute(1);
  asm volatile("s_waitcnt lgkmcnt(0)" ::: "memory");
  BAR();

  char* stgw = smem + w * 9216;
  {
#pragma unroll
    for (int pp = 0; pp < NPAIR; ++pp)
#pragma unroll
      for (int mi = 0; mi < 2; ++mi) {
        const f32x4 ga = acc[mi][G0 + 2 * pp], up = acc[mi][G0 + 2 * pp + 1];
        *reinterpret_cast<bf16x4*>(stgw + (mi * 16 + r16) * 144 + pp * 32 + g * 8) =
            pack4(fmaxf(ga[0], 0.f) * up[0], fmaxf(ga[1], 0.f) * up[1], fmaxf(ga[2], 0.f) * up[2], fmaxf(ga[3], 0.f) * up[3]);
      }
  }
  bf16x4 wf[2];
#pragma unroll
  for (int hh = 0; hh < 2; ++hh)
#pragma unroll
    for (int jj = 0; jj < 4; ++jj) wf[hh][jj] = (short)wphi[(2 * j + hh) * 256 + (4 * g + jj) * 16 + r16];

  if (TYPE == 0) {
#pragma unroll
    for (int hh = 0; hh < 2; ++hh) {
      f32x4 p[2]; float mx[2], mn[2], s1[2], s2[2];
#pragma unroll
      for (int mi = 0; mi < 2; ++mi) {
        const bf16x4 qf = pack4(acc[mi][hh][0], acc[mi][hh][1], acc[mi][hh][2], acc[mi][hh][3]);
        p[mi] = __builtin_amdgcn_mfma_f32_16x16x16bf16_1k(wf[hh], qf, (f32x4){0.f, 0.f, 0.f, 0.f}, 0, 0, 0);
      }
#pragma unroll
      for (int mi = 0; mi < 2; ++mi) {
        mx[mi] = xrow_max(fmaxf(fmaxf(p[mi][0], p[mi][1]), fmaxf(p[mi][2], p[mi][3])));
        mn[mi] = -xrow_max(-fminf(fminf(p[mi][0], p[mi][1]), fminf(p[mi][2], p[mi][3])));
      }
      f32x4 e1[2], e2[2];
#pragma unroll
      for (int mi = 0; mi < 2; ++mi) {
#pragma unroll
        for (int jj = 0; jj < 4; ++jj) { e1[mi][jj] = __expf(p[mi][jj] - mx[mi]); e2[mi][jj] = __expf(mn[mi] - p[mi][jj]); }
        s1[mi] = xrow_sum(e1[mi][0] + e1[mi][1] + e1[mi][2] + e1[mi][3]);
        s2[mi] = xrow_sum(e2[mi][0] + e2[mi][1] + e2[mi][2] + e2[mi][3]);
      }
#pragma unroll
      for (int mi = 0; mi < 2; ++mi) {
        const float r1 = __builtin_amdgcn_rcpf(s1[mi]), r2 = __builtin_amdgcn_rcpf(s2[mi]);
        char* dst = stgw + 4608 + (mi * 16 + r16) * 144 + hh * 64 + g * 8;
        *reinterpret_cast<bf16x4*>(dst) = pack4(e1[mi][0] * r1, e1[mi][1] * r1, e1[mi][2] * r1, e1[mi][3] * r1);
        *reinterpret_cast<bf16x4*>(dst + 32) = pack4(e2[mi][0] * r2, e2[mi][1] * r2, e2[mi][2] * r2, e2[mi][3] * r2);
      }
    }
    asm volatile("s_waitcnt lgkmcnt(0)" ::: "memory");
    __builtin_amdgcn_wave_barrier();
#pragma unroll
    for (int i = 0; i < 4; ++i) {
      const int tk = i * 8 + (l >> 3), ch = l & 7;
      const bf16x8 v = *reinterpret_cast<const bf16x8*>(stgw + tk * 144 + ch * 16);
      *reinterpret_cast<bf16x8*>(hbuf + (size_t)(m0 + w * 32 + tk) * F + f0 + ch * 8) = v;
      const bf16x8 v2 = *reinterpret_cast<const bf16x8*>(stgw + 4608 + tk * 144 + ch * 16);
      *reinterpret_cast<bf16x8*>(phiq + (size_t)(m0 + w * 32 + tk) * 1024 + j * 64 + ch * 8) = v2;
    }
  } else {
    float* red = reinterpret_cast<float*>(smem + 4 * 9216);
#pragma unroll
    for (int hh = 0; hh < 2; ++hh) {
      f32x4 p[2];
#pragma unroll
      for (int mi = 0; mi < 2; ++mi) {
        const bf16x4 kf = pack4(acc[mi][hh][0], acc[mi][hh][1], acc[mi][hh][2], acc[mi][hh][3]);
        p[mi] = __builtin_amdgcn_mfma_f32_16x16x16bf16_1k(kf, wf[hh], (f32x4){0.f, 0.f, 0.f, 0.f}, 0, 0, 0);
      }
      f32x4 mx[2], mn[2], e1[2], e2[2];
#pragma unroll
      for (int mi = 0; mi < 2; ++mi)
#pragma unroll
        for (int jj = 0; jj < 4; ++jj) { mx[mi][jj] = red16_max(p[mi][jj]); mn[mi][jj] = -red16_max(-p[mi][jj]); }
#pragma unroll
      for (int mi = 0; mi < 2; ++mi)
#pragma unroll
        for (int jj = 0; jj < 4; ++jj) { e1[mi][jj] = __expf(p[mi][jj] - mx[mi][jj]); e2[mi][jj] = __expf(mn[mi][jj] - p[mi][jj]); }
      float zp = 0.f, zn = 0.f;
#pragma unroll
      for (int mi = 0; mi < 2; ++mi)
#pragma unroll
        for (int jj = 0; jj < 4; ++jj) {
          const float s1 = red16_sum(e1[mi][jj]), s2 = red16_sum(e2[mi][jj]);
          e1[mi][jj] = bf2f(f2bf(e1[mi][jj] * __builtin_amdgcn_rcpf(s1)));
          e2[mi][jj] = bf2f(f2bf(e2[mi][jj] * __builtin_amdgcn_rcpf(s2)));
          zp += e1[mi][jj]; zn += e2[mi][jj];
        }
      f32x4 kvp = {0.f, 0.f, 0.f, 0.f}, kvn = {0.f, 0.f, 0.f, 0.f};
#pragma unroll
      for (int mi = 0; mi < 2; ++mi) {
        const bf16x4 vf = pack4(acc[mi][2 + hh][0], acc[mi][2 + hh][1], acc[mi][2 + hh][2], acc[mi][2 + hh][3]);
        kvp = __builtin_amdgcn_mfma_f32_16x16x16bf16_1k(pack4(e1[mi][0], e1[mi][1], e1[mi][2], e1[mi][3]), vf, kvp, 0, 0, 0);
        kvn = __builtin_amdgcn_mfma_f32_16x16x16bf16_1k(pack4(e2[mi][0], e2[mi][1], e2[mi][2], e2[mi][3]), vf, kvn, 0, 0, 0);
      }
      zp = xrow_sum(zp); zn = xrow_sum(zn);
      float* rw = red + w * 1088 + hh * 544;
#pragma unroll
      for (int jj = 0; jj < 4; ++jj) {
        rw[(4 * g + jj) * 16 + r16] = kvp[jj];
        rw[(16 + 4 * g + jj) * 16 + r16] = kvn[jj];
      }
      if (l < 16) { rw[512 + l] = zp; rw[528 + l] = zn; }
    }
    __syncthreads();
    unsigned long long* pdst = reinterpret_cast<unsigned long long*>(part + (size_t)(j * 16 + mt) * 1088);
    for (int idx = tid; idx < 544; idx += 256) {
      float a = 0.f, b = 0.f;
#pragma unroll
      for (int ww = 0; ww < 4; ++ww) { a += red[ww * 1088 + idx * 2]; b += red[ww * 1088 + idx * 2 + 1]; }
      const unsigned long long pk = ((unsigned long long)__float_as_uint(b) << 32) | __float_as_uint(a);
      __hip_atomic_store(pdst + idx, pk, __ATOMIC_RELAXED, __HIP_MEMORY_SCOPE_AGENT);
    }
    asm volatile("s_waitcnt vmcnt(0)" ::: "memory");
    __syncthreads();
    unsigned* flag = reinterpret_cast<unsigned*>(smem + 4 * 9216 + 4 * 1088 * 4);
    if (tid == 0) *flag = __hip_atomic_fetch_add(cnt + j, 1u, __ATOMIC_RELAXED, __HIP_MEMORY_SCOPE_AGENT);
#pragma unroll
    for (int i = 0; i < 3; ++i) {
      const int idx = i * 64 + l, tk = idx / 6, ch = idx - tk * 6;
      const bf16x8 v = *reinterpret_cast<const bf16x8*>(stgw + tk * 144 + ch * 16);
      *reinterpret_cast<bf16x8*>(hbuf + (size_t)(m0 + w * 32 + tk) * F + f0 + ch * 8) = v;
    }
    __syncthreads();
    if (*flag == 15u) {
      const unsigned long long* psrc = reinterpret_cast<const unsigned long long*>(part + (size_t)j * 16 * 1088);
      for (int idx = tid; idx < 544; idx += 256) {
        unsigned long long t[16];
#pragma unroll
        for (int m = 0; m < 16; ++m) t[m] = __hip_atomic_load(psrc + m * 544 + idx, __ATOMIC_RELAXED, __HIP_MEMORY_SCOPE_AGENT);
        float a = 0.f, b = 0.f;
#pragma unroll
        for (int m = 0; m < 16; ++m) { a += __uint_as_float((unsigned)t[m]); b += __uint_as_float((unsigned)(t[m] >> 32)); }
        const int hh = idx >= 272, e = idx * 2 - hh * 544, h = 2 * j + hh;
        if (e < 512) {
          const int f = e >> 4, d = e & 15;
          kvb[h * 512 + d * 32 + f] = f2bf(a); kvb[h * 512 + (d + 1) * 32 + f] = f2bf(b);
        } else {
          kvb[H * 512 + h * 32 + (e - 512)] = f2bf(a); kvb[H * 512 + h * 32 + (e - 511)] = f2bf(b);
        }
      }
    }
  }
}

__global__ __launch_bounds__(256, 2) void gemm1_kernel(const u16* __restrict__ xn, const u16* __restrict__ wqkv,
                                                       const u16* __restrict__ wphi, const u16* __restrict__ wg,
                                                       const u16* __restrict__ wu, u16* __restrict__ phiq,
                                                       u16* __restrict__ hbuf, float* __restrict__ part, unsigned* __restrict__ cnt,
                                                       u16* __restrict__ kvb) {
  __shared__ __attribute__((aligned(16))) char smem[2 * G1_STAGE];
  const int bid = blockIdx.x;
  const int xcd = bid & 7, idx = bid >> 3;
  const int mt = (xcd & 1) * 8 + (idx & 7), cb = (xcd >> 1) * 8 + (idx >> 3);
  if (cb & 1) gemm1_body<1>(smem, mt, cb >> 1, xn, wqkv, wphi, wg, wu, phiq, hbuf, part, cnt, kvb);
  else gemm1_body<0>(smem, mt, cb >> 1, xn, wqkv, wphi, wg, wu, phiq, hbuf, part, cnt, kvb);
}

constexpr int G2_OBYTES = 8 * 64 * 128;
constexpr int G2_ABYTES = 64 * 128;
constexpr int G2_BBYTES = 64 * 128;
constexpr int G2_STAGE = G2_ABYTES + G2_BBYTES;
constexpr int G2_SMEM = G2_OBYTES + 4 * G2_STAGE;

__global__ __launch_bounds__(256, 1) void gemm2_kernel(const u16* __restrict__ x, const u16* __restrict__ phiq,
                                                       const u16* __restrict__ hbuf, const u16* __restrict__ kvb,
                                                       const u16* __restrict__ wo, const u16* __restrict__ wd,
                                                       u16* __restrict__ out) {
  __shared__ __attribute__((aligned(16))) char smem[G2_SMEM];
  char* oimg = smem;
  char* stg = smem + G2_OBYTES;
  const int tid = threadIdx.x, w = tid >> 6, l = tid & 63, g = l >> 4, r16 = l & 15;
  const int bid = blockIdx.x;
  const int mt = bid & 31, nt = bid >> 5;
  const int m0 = mt * 64, n0 = nt * 64;

  const int wk = w >> 1, wn = w & 1;
  int brow[2], bcol[2], arow[2], acol[2];
#pragma unroll
  for (int i = 0; i < 2; ++i) {
    brow[i] = w * 16 + i * 8 + (l >> 3);
    const int s = ((brow[i] >> 1) & 1) | ((brow[i] >> 2) & 2);
    bcol[i] = n0 + (((l & 7) ^ (s << 1)) << 3);
    arow[i] = w * 16 + i * 8 + (l >> 3);
    acol[i] = ((l & 7) ^ ((arow[i] >> 1) & 7)) << 3;
  }
  auto stage = [&](int buf, int st) {
    char* sa = stg + buf * G2_STAGE; char* sb = sa + G2_ABYTES;
    const u16* wsrc = (st < 8) ? (wo + (size_t)st * 64 * D) : (wd + (size_t)(st - 8) * 64 * D);
    const u16* asrc = (st < 8) ? hbuf : (hbuf + (st - 8) * 64);
#pragma unroll
    for (int i = 0; i < 2; ++i) glds16(wsrc + (size_t)brow[i] * D + bcol[i], sb + (w * 16 + i * 8) * 128);
#pragma unroll
    for (int i = 0; i < 2; ++i) glds16(asrc + (size_t)(m0 + arow[i]) * F + acol[i], sa + (w * 16 + i * 8) * 128);
  };
  stage(0, 0); stage(1, 1); stage(2, 2);

  {
    bf16x8 kvf[8], zf[8], pf[8][4];
#pragma unroll
    for (int hh = 0; hh < 8; ++hh) {
      const int h = w * 8 + hh;
      kvf[hh] = *reinterpret_cast<const bf16x8*>(kvb + h * 512 + r16 * 32 + 8 * g);
      zf[hh] = *reinterpret_cast<const bf16x8*>(kvb + H * 512 + h * 32 + 8 * g);
#pragma unroll
      for (int mi = 0; mi < 4; ++mi)
        pf[hh][mi] = *reinterpret_cast<const bf16x8*>(phiq + (size_t)(m0 + mi * 16 + r16) * 1024 + h * 32 + 8 * g);
    }
#pragma unroll
    for (int hh = 0; hh < 8; ++hh) {
      const int h = w * 8 + hh;
#pragma unroll
      for (int mi = 0; mi < 4; ++mi) {
        const int row = mi * 16 + r16;
        f32x4 num = __builtin_amdgcn_mfma_f32_16x16x32_bf16(kvf[hh], pf[hh][mi], (f32x4){0.f, 0.f, 0.f, 0.f}, 0, 0, 0);
        f32x4 den = __builtin_amdgcn_mfma_f32_16x16x32_bf16(zf[hh], pf[hh][mi], (f32x4){0.f, 0.f, 0.f, 0.f}, 0, 0, 0);
        const float rd = __builtin_amdgcn_rcpf(den[0] + 1e-6f);
        const int st = h >> 2, ch = (h & 3) * 2 + (g >> 1);
        *reinterpret_cast<bf16x4*>(oimg + st * (64 * 128) + row * 128 + ((ch ^ ((row >> 1) & 7)) << 4) + (g & 1) * 8) =
            pack4(num[0] * rd, num[1] * rd, num[2] * rd, num[3] * rd);
      }
    }
  }
  asm volatile("s_waitcnt lgkmcnt(0)" ::: "memory");

  f32x4 acc[4][2];
#pragma unroll
  for (int a = 0; a < 4; ++a)
#pragma unroll
    for (int b = 0; b < 2; ++b) acc[a][b] = (f32x4){0.f, 0.f, 0.f, 0.f};
  const int q4 = r16 >> 2, p4 = l & 3;
  auto compute = [&](int buf, int st) {
    const char* sa = (st < 8) ? (oimg + st * (64 * 128)) : (stg + buf * G2_STAGE);
    const char* sb = stg + buf * G2_STAGE + G2_ABYTES;
    bf16x8 xa[4], wb[2];
#pragma unroll
    for (int mi = 0; mi < 4; ++mi) {
      const int row = mi * 16 + r16;
      xa[mi] = *reinterpret_cast<const bf16x8*>(sa + row * 128 + (((wk * 4 + g) ^ ((row >> 1) & 7)) << 4));
    }
#pragma unroll
    for (int ni = 0; ni < 2; ++ni) {
      bf16x4 t[2];
#pragma unroll
      for (int tt = 0; tt < 2; ++tt) {
        const int row = 32 * wk + 8 * g + 4 * tt + q4;
        const int s = ((row >> 1) & 1) | ((row >> 2) & 2);
        const int ch = 2 * (wn * 2 + ni) + (p4 >> 1);
        t[tt] = tr_read(sb + 128 * row + ((ch ^ (s << 1)) << 4) + 8 * (p4 & 1));
      }
      wb[ni] = cat8(t[0], t[1]);
    }
#pragma unroll
    for (int mi = 0; mi < 4; ++mi)
#pragma unroll
      for (int ni = 0; ni < 2; ++ni)
        acc[mi][ni] = __builtin_amdgcn_mfma_f32_16x16x32_bf16(wb[ni], xa[mi], acc[mi][ni], 0, 0, 0);
  };

#pragma unroll 1
  for (int st = 0; st < 33; ++st) {
    WAIT_V(8); BAR();
    stage((st + 3) & 3, st + 3);
    compute(st & 3, st);
  }
  WAIT_V(8); BAR(); compute(1, 33);
  WAIT_V(4); BAR(); compute(2, 34);
  WAIT_V(0); BAR(); compute(3, 35);
  __syncthreads();

  float* red = reinterpret_cast<float*>(stg);
#pragma unroll
  for (int mi = 0; mi < 4; ++mi)
#pragma unroll
    for (int ni = 0; ni < 2; ++ni)
      *reinterpret_cast<f32x4*>(red + wk * 4096 + (mi * 16 + r16) * 64 + (wn * 2 + ni) * 16 + 4 * g) = acc[mi][ni];
  __syncthreads();
  const int tok = tid >> 2, nc = (tid & 3) * 16;
  float o[16];
#pragma unroll
  for (int j = 0; j < 4; ++j) {
    f32x4 a = *reinterpret_cast<const f32x4*>(red + tok * 64 + nc + j * 4);
#pragma unroll
    for (int ww = 1; ww < 2; ++ww) {
      f32x4 b = *reinterpret_cast<const f32x4*>(red + ww * 4096 + tok * 64 + nc + j * 4);
      a += b;
    }
    o[j * 4 + 0] = a[0]; o[j * 4 + 1] = a[1]; o[j * 4 + 2] = a[2]; o[j * 4 + 3] = a[3];
  }
  const size_t go = (size_t)(m0 + tok) * D + n0 + nc;
#pragma unroll
  for (int hlf = 0; hlf < 2; ++hlf) {
    const bf16x8 xv = *reinterpret_cast<const bf16x8*>(x + go + hlf * 8);
    bf16x8 r;
#pragma unroll
    for (int j = 0; j < 8; ++j) r[j] = (short)f2bf(bf2f((u16)xv[j]) + o[hlf * 8 + j]);
    *reinterpret_cast<bf16x8*>(out + go + hlf * 8) = r;
  }
}

extern "C" void kernel_launch(void* const* d_in, const int* in_sizes, int n_in,
                              void* d_out, int out_size, void* d_ws, size_t ws_size,
                              hipStream_t stream) {
  const u16* x = (const u16*)d_in[0];
  const u16* wqkv = (const u16*)d_in[1];
  const u16* wphi = (const u16*)d_in[2];
  const u16* wo = (const u16*)d_in[3];
  const u16* wg = (const u16*)d_in[5];
  const u16* wu = (const u16*)d_in[6];
  const u16* wd = (const u16*)d_in[7];
  char* ws = (char*)d_ws;
  u16* xn = (u16*)(ws + WS_XN);
  u16* phiq = (u16*)(ws + WS_PHIQ);
  u16* hbuf = (u16*)(ws + WS_H);
  unsigned* cnt = (unsigned*)(ws + WS_CNT);
  u16* kvb = (u16*)(ws + WS_KVB);
  float* part = (float*)(ws + WS_PART);

  prep_kernel<<<S / 4, 256, 0, stream>>>(x, xn, cnt);
  gemm1_kernel<<<512, 256, 0, stream>>>(xn, wqkv, wphi, wg, wu, phiq, hbuf, part, cnt, kvb);
  static bool attr_set = false;
  if (!attr_set) {
    hipFuncSetAttribute((const void*)gemm2_kernel, hipFuncAttributeMaxDynamicSharedMemorySize, G2_SMEM);
    attr_set = true;
  }
  gemm2_kernel<<<256, 256, 0, stream>>>(x, phiq, hbuf, kvb, wo, wd, (u16*)d_out);
}
```

```cpp
#include <hip/hip_runtime.h>
#include <hip/hip_bf16.h>
#include <stdint.h>

typedef short bf16x8 __attribute__((ext_vector_type(8)));
typedef short bf16x4 __attribute__((ext_vector_type(4)));
typedef float f32x4 __attribute__((ext_vector_type(4)));
typedef unsigned short u16;
typedef __attribute__((address_space(3))) bf16x4* lds_b4_ptr;

constexpr int S = 2048, D = 512, H = 32, F = 1792;
constexpr int NQKV = 3 * D;

constexpr size_t WS_XN = 0;
constexpr size_t WS_PHIQ = WS_XN + (size_t)S * D * 2;
constexpr size_t WS_H = WS_PHIQ + (size_t)S * 1024 * 2;
constexpr size_t WS_CNT = WS_H + (size_t)S * F * 2;
constexpr size_t WS_KVB = WS_CNT + 256;
constexpr size_t WS_PART = WS_KVB + 40960;

__device__ __forceinline__ float bf2f(u16 v) { return __uint_as_float(((unsigned)v) << 16); }
__device__ __forceinline__ u16 f2bf(float f) {
  __hip_bfloat16 h = __float2bfloat16(f);
  return *reinterpret_cast<u16*>(&h);
}
__device__ __forceinline__ bf16x4 pack4(float a, float b, float c, float d) {
  bf16x4 r;
  r[0] = (short)f2bf(a); r[1] = (short)f2bf(b); r[2] = (short)f2bf(c); r[3] = (short)f2bf(d);
  return r;
}
__device__ __forceinline__ void glds16(const void* g, void* l) {
  unsigned keep;
  const unsigned dst = __builtin_amdgcn_readfirstlane((unsigned)(uintptr_t)l);
  asm volatile("s_mov_b32 %0, m0\n\ts_mov_b32 m0, %2\n\ts_nop 0\n\tglobal_load_lds_dwordx4 %1, off\n\ts_mov_b32 m0, %0"
               : "=&s"(keep) : "v"(g), "s"(dst) : "memory");
}
__device__ __forceinline__ bf16x4 tr_read(const char* p) {
  return __builtin_amdgcn_ds_read_tr16_b64_v4i16((lds_b4_ptr)p);
}
__device__ __forceinline__ bf16x8 cat8(bf16x4 a, bf16x4 b) {
  return (bf16x8){a[0], a[1], a[2], a[3], b[0], b[1], b[2], b[3]};
}
#define WAIT_V(n) asm volatile("s_waitcnt vmcnt(" #n ")" ::: "memory")
#define BAR() __builtin_amdgcn_s_barrier()
template <int CTRL> __device__ __forceinline__ float dppf(float v) {
  return __int_as_float(__builtin_amdgcn_update_dpp(0, __float_as_int(v), CTRL, 0xF, 0xF, true));
}
__device__ __forceinline__ float red16_max(float v) {
  v = fmaxf(v, dppf<0xB1>(v)); v = fmaxf(v, dppf<0x4E>(v));
  v = fmaxf(v, dppf<0x124>(v)); v = fmaxf(v, dppf<0x128>(v)); return v;
}
__device__ __forceinline__ float red16_sum(float v) {
  v += dppf<0xB1>(v); v += dppf<0x4E>(v);
  v += dppf<0x124>(v); v += dppf<0x128>(v); return v;
}

__device__ __forceinline__ float xrow_max(float v) {
  auto a = __builtin_amdgcn_permlane16_swap(__float_as_uint(v), __float_as_uint(v), false, false);
  v = fmaxf(__uint_as_float(a[0]), __uint_as_float(a[1]));
  auto b = __builtin_amdgcn_permlane32_swap(__float_as_uint(v), __float_as_uint(v), false, false);
  return fmaxf(__uint_as_float(b[0]), __uint_as_float(b[1]));
}
__device__ __forceinline__ float xrow_sum(float v) {
  auto a = __builtin_amdgcn_permlane16_swap(__float_as_uint(v), __float_as_uint(v), false, false);
  v = __uint_as_float(a[0]) + __uint_as_float(a[1]);
  auto b = __builtin_amdgcn_permlane32_swap(__float_as_uint(v), __float_as_uint(v), false, false);
  return __uint_as_float(b[0]) + __uint_as_float(b[1]);
}

__global__ __launch_bounds__(256) void prep_kernel(const u16* __restrict__ x, u16* __restrict__ xn, unsigned* __restrict__ cnt) {
  const int wave = (blockIdx.x * 256 + threadIdx.x) >> 6, lane = threadIdx.x & 63;
  const int gid = blockIdx.x * 256 + threadIdx.x;
  if (gid < 64) cnt[gid] = 0u;
  bf16x8 v = *reinterpret_cast<const bf16x8*>(x + (size_t)wave * D + lane * 8);
  float f[8]; float s = 0.f;
#pragma unroll
  for (int j = 0; j < 8; ++j) { f[j] = bf2f((u16)v[j]); s += f[j]; }
#pragma unroll
  for (int o = 32; o > 0; o >>= 1) s += __shfl_xor(s, o);
  const float mean = s * (1.f / D);
  float q = 0.f;
#pragma unroll
  for (int j = 0; j < 8; ++j) { f[j] -= mean; q += f[j] * f[j]; }
#pragma unroll
  for (int o = 32; o > 0; o >>= 1) q += __shfl_xor(q, o);
  const float rstd = rsqrtf(q * (1.f / D) + 1e-5f);
  bf16x8 r;
#pragma unroll
  for (int j = 0; j < 8; ++j) r[j] = (short)f2bf(f[j] * rstd);
  *reinterpret_cast<bf16x8*>(xn + (size_t)wave * D + lane * 8) = r;
}

constexpr int G1_ABYTES = 128 * 128;
constexpr int G1_BBYTES = 64 * 320;
constexpr int G1_STAGE = G1_ABYTES + G1_BBYTES;

template <int TYPE>
__device__ __forceinline__ void gemm1_body(char* smem, int mt, int j,
                                           const u16* __restrict__ xn, const u16* __restrict__ wqkv,
                                           const u16* __restrict__ wphi, const u16* __restrict__ wg,
                                           const u16* __restrict__ wu, u16* __restrict__ phiq,
                                           u16* __restrict__ hbuf, float* __restrict__ part, unsigned* __restrict__ cnt,
                                           u16* __restrict__ kvb) {
  const int tid = threadIdx.x, w = tid >> 6, l = tid & 63;
  const int g = l >> 4, r16 = l & 15;
  const int m0 = mt * 128;
  constexpr int NPAIR = (TYPE == 0) ? 4 : 3;
  constexpr int G0 = (TYPE == 0) ? 2 : 4;
  const int f0 = (TYPE == 0) ? j * 64 : 1024 + j * 48;

  const u16* asrc[4]; const u16* bsrc[5]; int bld[5];
#pragma unroll
  for (int i = 0; i < 4; ++i) {
    const int row = w * 32 + i * 8 + (l >> 3);
    asrc[i] = xn + (size_t)(m0 + row) * D + (((l & 7) ^ ((row >> 1) & 7)) << 3);
  }
#pragma unroll
  for (int i = 0; i < 5; ++i) {
    const int c = (w * 5 + i) * 64 + l, row = c / 20, chs = c - row * 20;
    const int ch = chs ^ (((row >> 3) & 1) << 1);
    const int ni = ch >> 1, off = (ch & 1) * 8;
    const u16* base; int ld;
    if (ni >= G0) { base = (((ni - G0) & 1) ? wu : wg) + f0 + ((ni - G0) >> 1) * 16 + off; ld = F; }
    else if (TYPE == 0) { base = wqkv + (2 * j + ni) * 16 + off; ld = NQKV; }
    else { base = wqkv + ((ni < 2) ? D : 2 * D) + (2 * j + (ni & 1)) * 16 + off; ld = NQKV; }
    bsrc[i] = base + (size_t)row * ld; bld[i] = 64 * ld;
  }
  auto stage = [&](int buf, int kt) {
    char* sa = smem + buf * G1_STAGE; char* sb = sa + G1_ABYTES;
#pragma unroll
    for (int i = 0; i < 4; ++i) glds16(asrc[i] + kt * 64, sa + (w * 32 + i * 8) * 128);
#pragma unroll
    for (int i = 0; i < 5; ++i) glds16(bsrc[i] + (size_t)kt * bld[i], sb + (w * 5 + i) * 1024);
  };

  f32x4 acc[2][10];
#pragma unroll
  for (int a = 0; a < 2; ++a)
#pragma unroll
    for (int b = 0; b < 10; ++b) acc[a][b] = (f32x4){0.f, 0.f, 0.f, 0.f};

  const int q4 = r16 >> 2, p4 = l & 3;
  auto compute = [&](int buf) {
    const char* sa = smem + buf * G1_STAGE; const char* sb = sa + G1_ABYTES;
#pragma unroll
    for (int kk = 0; kk < 2; ++kk) {
      bf16x8 xa[2];
#pragma unroll
      for (int mi = 0; mi < 2; ++mi) {
        const int row = w * 32 + mi * 16 + r16;
        xa[mi] = *reinterpret_cast<const bf16x8*>(sa + row * 128 + (((kk * 4 + g) ^ ((row >> 1) & 7)) << 4));
      }
#pragma unroll
      for (int ni = 0; ni < 10; ++ni) {
        bf16x4 t[2];
#pragma unroll
        for (int tt = 0; tt < 2; ++tt) {
          const int row = 32 * kk + 8 * g + 4 * tt + q4;
          const int ch = 2 * ni + (p4 >> 1);
          t[tt] = tr_read(sb + 320 * row + ((ch ^ (((row >> 3) & 1) << 1)) << 4) + 8 * (p4 & 1));
        }
        const bf16x8 wb = cat8(t[0], t[1]);
#pragma unroll
        for (int mi = 0; mi < 2; ++mi) {
          if (TYPE == 1 && (ni == 2 || ni == 3)) acc[mi][ni] = __builtin_amdgcn_mfma_f32_16x16x32_bf16(xa[mi], wb, acc[mi][ni], 0, 0, 0);
          else acc[mi][ni] = __builtin_amdgcn_mfma_f32_16x16x32_bf16(wb, xa[mi], acc[mi][ni], 0, 0, 0);
        }
      }
    }
  };

  stage(0, 0);
#pragma unroll 1
  for (int kt = 0; kt < 7; ++kt) {
    WAIT_V(0); BAR();
    stage((kt + 1) & 1, kt + 1);
    compute(kt & 1);
  }
  WAIT_V(0); BAR(); compute(1);
  asm volatile("s_waitcnt lgkmcnt(0)" ::: "memory");
  BAR();

  char* stgw = smem + w * 9216;
  {
#pragma unroll
    for (int pp = 0; pp < NPAIR; ++pp)
#pragma unroll
      for (int mi = 0; mi < 2; ++mi) {
        const f32x4 ga = acc[mi][G0 + 2 * pp], up = acc[mi][G0 + 2 * pp + 1];
        *reinterpret_cast<bf16x4*>(stgw + (mi * 16 + r16) * 144 + pp * 32 + g * 8) =
            pack4(fmaxf(ga[0], 0.f) * up[0], fmaxf(ga[1], 0.f) * up[1], fmaxf(ga[2], 0.f) * up[2], fmaxf(ga[3], 0.f) * up[3]);
      }
  }
  bf16x4 wf[2];
#pragma unroll
  for (int hh = 0; hh < 2; ++hh)
#pragma unroll
    for (int jj = 0; jj < 4; ++jj) wf[hh][jj] = (short)wphi[(2 * j + hh) * 256 + (4 * g + jj) * 16 + r16];

  if (TYPE == 0) {
#pragma unroll
    for (int hh = 0; hh < 2; ++hh) {
      f32x4 p[2]; float mx[2], mn[2], s1[2], s2[2];
#pragma unroll
      for (int mi = 0; mi < 2; ++mi) {
        const bf16x4 qf = pack4(acc[mi][hh][0], acc[mi][hh][1], acc[mi][hh][2], acc[mi][hh][3]);
        p[mi] = __builtin_amdgcn_mfma_f32_16x16x16bf16_1k(wf[hh], qf, (f32x4){0.f, 0.f, 0.f, 0.f}, 0, 0, 0);
      }
#pragma unroll
      for (int mi = 0; mi < 2; ++mi) {
        mx[mi] = xrow_max(fmaxf(fmaxf(p[mi][0], p[mi][1]), fmaxf(p[mi][2], p[mi][3])));
        mn[mi] = -xrow_max(-fminf(fminf(p[mi][0], p[mi][1]), fminf(p[mi][2], p[mi][3])));
      }
      f32x4 e1[2], e2[2];
#pragma unroll
      for (int mi = 0; mi < 2; ++mi) {
#pragma unroll
        for (int jj = 0; jj < 4; ++jj) { e1[mi][jj] = __expf(p[mi][jj] - mx[mi]); e2[mi][jj] = __expf(mn[mi] - p[mi][jj]); }
        s1[mi] = xrow_sum(e1[mi][0] + e1[mi][1] + e1[mi][2] + e1[mi][3]);
        s2[mi] = xrow_sum(e2[mi][0] + e2[mi][1] + e2[mi][2] + e2[mi][3]);
      }
#pragma unroll
      for (int mi = 0; mi < 2; ++mi) {
        const float r1 = __builtin_amdgcn_rcpf(s1[mi]), r2 = __builtin_amdgcn_rcpf(s2[mi]);
        char* dst = stgw + 4608 + (mi * 16 + r16) * 144 + hh * 64 + g * 8;
        *reinterpret_cast<bf16x4*>(dst) = pack4(e1[mi][0] * r1, e1[mi][1] * r1, e1[mi][2] * r1, e1[mi][3] * r1);
        *reinterpret_cast<bf16x4*>(dst + 32) = pack4(e2[mi][0] * r2, e2[mi][1] * r2, e2[mi][2] * r2, e2[mi][3] * r2);
      }
    }
    asm volatile("s_waitcnt lgkmcnt(0)" ::: "memory");
    __builtin_amdgcn_wave_barrier();
#pragma unroll
    for (int i = 0; i < 4; ++i) {
      const int tk = i * 8 + (l >> 3), ch = l & 7;
      const bf16x8 v = *reinterpret_cast<const bf16x8*>(stgw + tk * 144 + ch * 16);
      *reinterpret_cast<bf16x8*>(hbuf + (size_t)(m0 + w * 32 + tk) * F + f0 + ch * 8) = v;
      const bf16x8 v2 = *reinterpret_cast<const bf16x8*>(stgw + 4608 + tk * 144 + ch * 16);
      *reinterpret_cast<bf16x8*>(phiq + (size_t)(m0 + w * 32 + tk) * 1024 + j * 64 + ch * 8) = v2;
    }
  } else {
    float* red = reinterpret_cast<float*>(smem + 4 * 9216);
#pragma unroll
    for (int hh = 0; hh < 2; ++hh) {
      f32x4 p[2];
#pragma unroll
      for (int mi = 0; mi < 2; ++mi) {
        const bf16x4 kf = pack4(acc[mi][hh][0], acc[mi][hh][1], acc[mi][hh][2], acc[mi][hh][3]);
        p[mi] = __builtin_amdgcn_mfma_f32_16x16x16bf16_1k(kf, wf[hh], (f32x4){0.f, 0.f, 0.f, 0.f}, 0, 0, 0);
      }
      f32x4 mx[2], mn[2], e1[2], e2[2];
#pragma unroll
      for (int mi = 0; mi < 2; ++mi)
#pragma unroll
        for (int jj = 0; jj < 4; ++jj) { mx[mi][jj] = red16_max(p[mi][jj]); mn[mi][jj] = -red16_max(-p[mi][jj]); }
#pragma unroll
      for (int mi = 0; mi < 2; ++mi)
#pragma unroll
        for (int jj = 0; jj < 4; ++jj) { e1[mi][jj] = __expf(p[mi][jj] - mx[mi][jj]); e2[mi][jj] = __expf(mn[mi][jj] - p[mi][jj]); }
      float zp = 0.f, zn = 0.f;
#pragma unroll
      for (int mi = 0; mi < 2; ++mi)
#pragma unroll
        for (int jj = 0; jj < 4; ++jj) {
          const float s1 = red16_sum(e1[mi][jj]), s2 = red16_sum(e2[mi][jj]);
          e1[mi][jj] = bf2f(f2bf(e1[mi][jj] * __builtin_amdgcn_rcpf(s1)));
          e2[mi][jj] = bf2f(f2bf(e2[mi][jj] * __builtin_amdgcn_rcpf(s2)));
          zp += e1[mi][jj]; zn += e2[mi][jj];
        }
      f32x4 kvp = {0.f, 0.f, 0.f, 0.f}, kvn = {0.f, 0.f, 0.f, 0.f};
#pragma unroll
      for (int mi = 0; mi < 2; ++mi) {
        const bf16x4 vf = pack4(acc[mi][2 + hh][0], acc[mi][2 + hh][1], acc[mi][2 + hh][2], acc[mi][2 + hh][3]);
        kvp = __builtin_amdgcn_mfma_f32_16x16x16bf16_1k(pack4(e1[mi][0], e1[mi][1], e1[mi][2], e1[mi][3]), vf, kvp, 0, 0, 0);
        kvn = __builtin_amdgcn_mfma_f32_16x16x16bf16_1k(pack4(e2[mi][0], e2[mi][1], e2[mi][2], e2[mi][3]), vf, kvn, 0, 0, 0);
      }
      zp = xrow_sum(zp); zn = xrow_sum(zn);
      float* rw = red + w * 1088 + hh * 544;
#pragma unroll
      for (int jj = 0; jj < 4; ++jj) {
        rw[(4 * g + jj) * 16 + r16] = kvp[jj];
        rw[(16 + 4 * g + jj) * 16 + r16] = kvn[jj];
      }
      if (l < 16) { rw[512 + l] = zp; rw[528 + l] = zn; }
    }
    __syncthreads();
    unsigned long long* pdst = reinterpret_cast<unsigned long long*>(part + (size_t)(j * 16 + mt) * 1088);
    for (int idx = tid; idx < 544; idx += 256) {
      float a = 0.f, b = 0.f;
#pragma unroll
      for (int ww = 0; ww < 4; ++ww) { a += red[ww * 1088 + idx * 2]; b += red[ww * 1088 + idx * 2 + 1]; }
      const unsigned long long pk = ((unsigned long long)__float_as_uint(b) << 32) | __float_as_uint(a);
      __hip_atomic_store(pdst + idx, pk, __ATOMIC_RELAXED, __HIP_MEMORY_SCOPE_AGENT);
    }
    asm volatile("s_waitcnt vmcnt(0)" ::: "memory");
    __syncthreads();
    unsigned* flag = reinterpret_cast<unsigned*>(smem + 4 * 9216 + 4 * 1088 * 4);
    if (tid == 0) *flag = __hip_atomic_fetch_add(cnt + j, 1u, __ATOMIC_RELAXED, __HIP_MEMORY_SCOPE_AGENT);
#pragma unroll
    for (int i = 0; i < 3; ++i) {
      const int idx = i * 64 + l, tk = idx / 6, ch = idx - tk * 6;
      const bf16x8 v = *reinterpret_cast<const bf16x8*>(stgw + tk * 144 + ch * 16);
      *reinterpret_cast<bf16x8*>(hbuf + (size_t)(m0 + w * 32 + tk) * F + f0 + ch * 8) = v;
    }
    __syncthreads();
    if (*flag == 15u) {
      const unsigned long long* psrc = reinterpret_cast<const unsigned long long*>(part + (size_t)j * 16 * 1088);
      for (int idx = tid; idx < 544; idx += 256) {
        unsigned long long t[16];
#pragma unroll
        for (int m = 0; m < 16; ++m) t[m] = __hip_atomic_load(psrc + m * 544 + idx, __ATOMIC_RELAXED, __HIP_MEMORY_SCOPE_AGENT);
        float a = 0.f, b = 0.f;
#pragma unroll
        for (int m = 0; m < 16; ++m) { a += __uint_as_float((unsigned)t[m]); b += __uint_as_float((unsigned)(t[m] >> 32)); }
        const int hh = idx >= 272, e = idx * 2 - hh * 544, h = 2 * j + hh;
        if (e < 512) {
          const int f = e >> 4, d = e & 15;
          kvb[h * 512 + d * 32 + f] = f2bf(a); kvb[h * 512 + (d + 1) * 32 + f] = f2bf(b);
        } else {
          kvb[H * 512 + h * 32 + (e - 512)] = f2bf(a); kvb[H * 512 + h * 32 + (e - 511)] = f2bf(b);
        }
      }
    }
  }
}

__global__ __launch_bounds__(256, 2) void gemm1_kernel(const u16* __restrict__ xn, const u16* __restrict__ wqkv,
                                                       const u16* __restrict__ wphi, const u16* __restrict__ wg,
                                                       const u16* __restrict__ wu, u16* __restrict__ phiq,
                                                       u16* __restrict__ hbuf, float* __restrict__ part, unsigned* __restrict__ cnt,
                                                       u16* __restrict__ kvb) {
  __shared__ __attribute__((aligned(16))) char smem[2 * G1_STAGE];
  const int bid = blockIdx.x;
  const int xcd = bid & 7, idx = bid >> 3;
  const int mt = (xcd & 1) * 8 + (idx & 7), cb = (xcd >> 1) * 8 + (idx >> 3);
  if (cb & 1) gemm1_body<1>(smem, mt, cb >> 1, xn, wqkv, wphi, wg, wu, phiq, hbuf, part, cnt, kvb);
  else gemm1_body<0>(smem, mt, cb >> 1, xn, wqkv, wphi, wg, wu, phiq, hbuf, part, cnt, kvb);
}

constexpr int G2_OBYTES = 8 * 64 * 128;
constexpr int G2_ABYTES = 64 * 128;
constexpr int G2_BBYTES = 64 * 128;
constexpr int G2_STAGE = G2_ABYTES + G2_BBYTES;
constexpr int G2_SMEM = G2_OBYTES + 4 * G2_STAGE;

__global__ __launch_bounds__(512, 1) void gemm2_kernel(const u16* __restrict__ x, const u16* __restrict__ phiq,
                                                       const u16* __restrict__ hbuf, const u16* __restrict__ kvb,
                                                       const u16* __restrict__ wo, const u16* __restrict__ wd,
                                                       u16* __restrict__ out) {
  __shared__ __attribute__((aligned(16))) char smem[G2_SMEM];
  char* oimg = smem;
  char* stg = smem + G2_OBYTES;
  const int tid = threadIdx.x, w = tid >> 6, l = tid & 63, g = l >> 4, r16 = l & 15;
  const int bid = blockIdx.x;
  const int mt = bid & 31, nt = bid >> 5;
  const int m0 = mt * 64, n0 = nt * 64;
  const int wk = w >> 2, wn = (w >> 1) & 1, wm = w & 1;

  const int srow = w * 8 + (l >> 3);
  const int sB = ((srow >> 1) & 1) | ((srow >> 2) & 2);
  const int bcol = n0 + (((l & 7) ^ (sB << 1)) << 3);
  const int acol = ((l & 7) ^ ((srow >> 1) & 7)) << 3;
  auto stage = [&](int buf, int st) {
    char* sa = stg + buf * G2_STAGE; char* sb = sa + G2_ABYTES;
    const u16* wsrc = (st < 8) ? (wo + (size_t)st * 64 * D) : (wd + (size_t)(st - 8) * 64 * D);
    const u16* asrc = (st < 8) ? hbuf : (hbuf + (st - 8) * 64);
    glds16(wsrc + (size_t)srow * D + bcol, sb + w * 8 * 128);
    glds16(asrc + (size_t)(m0 + srow) * F + acol, sa + w * 8 * 128);
  };
  {
    bf16x8 kvf[4], zf[4], pf[4][4];
#pragma unroll
    for (int hh = 0; hh < 4; ++hh) {
      const int h = w * 4 + hh;
      kvf[hh] = *reinterpret_cast<const bf16x8*>(kvb + h * 512 + r16 * 32 + 8 * g);
      zf[hh] = *reinterpret_cast<const bf16x8*>(kvb + H * 512 + h * 32 + 8 * g);
#pragma unroll
      for (int mi = 0; mi < 4; ++mi)
        pf[hh][mi] = *reinterpret_cast<const bf16x8*>(phiq + (size_t)(m0 + mi * 16 + r16) * 1024 + h * 32 + 8 * g);
    }
    stage(0, 0); stage(1, 1); stage(2, 2);
#pragma unroll
    for (int hh = 0; hh < 4; ++hh) {
      const int h = w * 4 + hh;
#pragma unroll
      for (int mi = 0; mi < 4; ++mi) {
        const int row = mi * 16 + r16;
        f32x4 num = __builtin_amdgcn_mfma_f32_16x16x32_bf16(kvf[hh], pf[hh][mi], (f32x4){0.f, 0.f, 0.f, 0.f}, 0, 0, 0);
        f32x4 den = __builtin_amdgcn_mfma_f32_16x16x32_bf16(zf[hh], pf[hh][mi], (f32x4){0.f, 0.f, 0.f, 0.f}, 0, 0, 0);
        const float rd = __builtin_amdgcn_rcpf(den[0] + 1e-6f);
        const int st = h >> 2, ch = (h & 3) * 2 + (g >> 1);
        *reinterpret_cast<bf16x4*>(oimg + st * (64 * 128) + row * 128 + ((ch ^ ((row >> 1) & 7)) << 4) + (g & 1) * 8) =
            pack4(num[0] * rd, num[1] * rd, num[2] * rd, num[3] * rd);
      }
    }
  }
  asm volatile("s_waitcnt lgkmcnt(0)" ::: "memory");

  f32x4 acc[2][2];
#pragma unroll
  for (int a = 0; a < 2; ++a)
#pragma unroll
    for (int b = 0; b < 2; ++b) acc[a][b] = (f32x4){0.f, 0.f, 0.f, 0.f};
  const int q4 = r16 >> 2, p4 = l & 3;
  auto compute = [&](int buf, int st) {
    const char* sa = (st < 8) ? (oimg + st * (64 * 128)) : (stg + buf * G2_STAGE);
    const char* sb = stg + buf * G2_STAGE + G2_ABYTES;
    bf16x8 xa[2], wb[2];
#pragma unroll
    for (int mi = 0; mi < 2; ++mi) {
      const int row = wm * 32 + mi * 16 + r16;
      xa[mi] = *reinterpret_cast<const bf16x8*>(sa + row * 128 + (((wk * 4 + g) ^ ((row >> 1) & 7)) << 4));
    }
#pragma unroll
    for (int ni = 0; ni < 2; ++ni) {
      bf16x4 t[2];
#pragma unroll
      for (int tt = 0; tt < 2; ++tt) {
        const int row = 32 * wk + 8 * g + 4 * tt + q4;
        const int s = ((row >> 1) & 1) | ((row >> 2) & 2);
        const int ch = 2 * (wn * 2 + ni) + (p4 >> 1);
        t[tt] = tr_read(sb + 128 * row + ((ch ^ (s << 1)) << 4) + 8 * (p4 & 1));
      }
      wb[ni] = cat8(t[0], t[1]);
    }
#pragma unroll
    for (int mi = 0; mi < 2; ++mi)
#pragma unroll
      for (int ni = 0; ni < 2; ++ni)
        acc[mi][ni] = __builtin_amdgcn_mfma_f32_16x16x32_bf16(wb[ni], xa[mi], acc[mi][ni], 0, 0, 0);
  };

#pragma unroll 1
  for (int st = 0; st < 33; ++st) {
    WAIT_V(4); BAR();
    stage((st + 3) & 3, st + 3);
    compute(st & 3, st);
  }
  WAIT_V(4); BAR(); compute(1, 33);
  WAIT_V(2); BAR(); compute(2, 34);
  WAIT_V(0); BAR(); compute(3, 35);
  __syncthreads();

  float* red = reinterpret_cast<float*>(stg);
#pragma unroll
  for (int mi = 0; mi < 2; ++mi)
#pragma unroll
    for (int ni = 0; ni < 2; ++ni)
      *reinterpret_cast<f32x4*>(red + wk * 4096 + (wm * 32 + mi * 16 + r16) * 64 + (wn * 2 + ni) * 16 + 4 * g) = acc[mi][ni];
  __syncthreads();
  const int tok = tid >> 3, nc = (tid & 7) * 8;
  f32x4 a0 = *reinterpret_cast<const f32x4*>(red + tok * 64 + nc) + *reinterpret_cast<const f32x4*>(red + 4096 + tok * 64 + nc);
  f32x4 a1 = *reinterpret_cast<const f32x4*>(red + tok * 64 + nc + 4) + *reinterpret_cast<const f32x4*>(red + 4096 + tok * 64 + nc + 4);
  const size_t go = (size_t)(m0 + tok) * D + n0 + nc;
  const bf16x8 xv = *reinterpret_cast<const bf16x8*>(x + go);
  bf16x8 r;
#pragma unroll
  for (int j = 0; j < 4; ++j) { r[j] = (short)f2bf(bf2f((u16)xv[j]) + a0[j]); r[4 + j] = (short)f2bf(bf2f((u16)xv[4 + j]) + a1[j]); }
  *reinterpret_cast<bf16x8*>(out + go) = r;
}

extern "C" void kernel_launch(void* const* d_in, const int* in_sizes, int n_in,
                              void* d_out, int out_size, void* d_ws, size_t ws_size,
                              hipStream_t stream) {
  const u16* x = (const u16*)d_in[0];
  const u16* wqkv = (const u16*)d_in[1];
  const u16* wphi = (const u16*)d_in[2];
  const u16* wo = (const u16*)d_in[3];
  const u16* wg = (const u16*)d_in[5];
  const u16* wu = (const u16*)d_in[6];
  const u16* wd = (const u16*)d_in[7];
  char* ws = (char*)d_ws;
  u16* xn = (u16*)(ws + WS_XN);
  u16* phiq = (u16*)(ws + WS_PHIQ);
  u16* hbuf = (u16*)(ws + WS_H);
  unsigned* cnt = (unsigned*)(ws + WS_CNT);
  u16* kvb = (u16*)(ws + WS_KVB);
  float* part = (float*)(ws + WS_PART);

  prep_kernel<<<S / 4, 256, 0, stream>>>(x, xn, cnt);
  gemm1_kernel<<<512, 256, 0, stream>>>(xn, wqkv, wphi, wg, wu, phiq, hbuf, part, cnt, kvb);
  static bool attr_set = false;
  if (!attr_set) {
    hipFuncSetAttribute((const void*)gemm2_kernel, hipFuncAttributeMaxDynamicSharedMemorySize, G2_SMEM);
    attr_set = true;
  }
  gemm2_kernel<<<256, 512, 0, stream>>>(x, phiq, hbuf, kvb, wo, wd, (u16*)d_out);
}
```
